```python
import torch

def _softmax(x, axis=-1):
    return torch.softmax(x, dim=axis)
def _gelu(x):
    return torch.nn.functional.gelu(x, approximate="tanh")
def _silu(x):
    return torch.nn.functional.silu(x)
def _sigmoid(x):
    return torch.sigmoid(x)
def _layernorm(x, axis=-1, eps=1e-5):
    m = x.mean(dim=axis, keepdim=True)
    v = x.var(dim=axis, keepdim=True, unbiased=False)
    return (x - m) / torch.sqrt(v + eps)
def _scatter_add(tgt, idx, src, axis=0):
    return tgt.clone().index_add_(axis, idx, src)
def _segment_sum(src, seg, n):
    out = torch.zeros((n,) + src.shape[1:], dtype=src.dtype)
    return out.index_add_(0, seg, src)
def _pad(x, pw):
    flat = tuple(v for pair in reversed(pw) for v in pair)
    return torch.nn.functional.pad(x, flat)

kernel_name = 'gated_mix__linear_attn_block__ssm_conv_s4__prompt'

def setup_inputs():

    _w = (lambda a: (lambda t: t.to(torch.bfloat16) if t.is_floating_point() else t)(torch.as_tensor(a)))
    def _A_setup():
        B, S, D = 1, 2048, 576
        return {
            "x": _w((lambda *s: torch.randn(*s, dtype=torch.bfloat16) * 0.05)(B, S, D)),
            "wqkv": _w((lambda *s: torch.randn(*s, dtype=torch.bfloat16) * 0.8)(D, 3 * D)),
            "wo": _w((lambda *s: torch.randn(*s, dtype=torch.bfloat16) * 0.05)(D, D)),
        }
    _A_in = _A_setup()

    def _B_setup():
        B, L, D, N = 1, 2048, 576, 128
        A_re = -(lambda _x: torch.exp(torch.as_tensor(_x)))((lambda *s: torch.randn(*s, dtype=torch.bfloat16) * 3.2)(D, N))
        A_im = torch.pi * (lambda *s: torch.randn(*s, dtype=torch.bfloat16) * 0.05)(D, N)
        B_re = (lambda *s: torch.randn(*s, dtype=torch.bfloat16) * 0.2)(D, N)
        B_im = (lambda *s: torch.randn(*s, dtype=torch.bfloat16) * 0.2)(D, N)
        C_re = (lambda *s: torch.randn(*s, dtype=torch.bfloat16) * 0.2)(D, N)
        C_im = (lambda *s: torch.randn(*s, dtype=torch.bfloat16) * 0.2)(D, N)
        log_dt = (lambda *s: torch.randn(*s, dtype=torch.bfloat16) * 0.05)(D) * 0.5 - 4.0
        Dskip = (lambda *s: torch.randn(*s, dtype=torch.bfloat16) * 0.05)(D)
        return {
            "u": _w((lambda *s: torch.randn(*s, dtype=torch.bfloat16) * 0.05)(B, L, D)),
            "A_re": _w(A_re),
            "A_im": _w(A_im),
            "B_re": _w(B_re),
            "B_im": _w(B_im),
            "C_re": _w(C_re),
            "C_im": _w(C_im),
            "log_dt": _w(log_dt),
            "Dskip": _w(Dskip),
        }
    _B_in = _B_setup()

    return {
        "A_x": _A_in["x"],
        "A_wqkv": _A_in["wqkv"],
        "A_wo": _A_in["wo"],
        "B_u": _B_in["u"],
        "B_A_re": _B_in["A_re"],
        "B_A_im": _B_in["A_im"],
        "B_B_re": _B_in["B_re"],
        "B_B_im": _B_in["B_im"],
        "B_C_re": _B_in["C_re"],
        "B_C_im": _B_in["C_im"],
        "B_log_dt": _B_in["log_dt"],
        "B_Dskip": _B_in["Dskip"],
        "gate_w": _w((lambda *s: torch.randn(*s, dtype=torch.bfloat16) * 0.05)(576) * 16.0),
    }

def reference(A_x, A_wqkv, A_wo, B_u, B_A_re, B_A_im, B_B_re, B_B_im, B_C_re, B_C_im, B_log_dt, B_Dskip, gate_w):
    def _A_ref(x, wqkv, wo):
        B, S, D = x.shape
        H = 32; Dh = D // H
        qkv = (lambda x, *d: x.permute(*d))((x @ wqkv).reshape(B, S, 3, H, Dh), 2, 0, 3, 1, 4)
        q, k, v = qkv[0], qkv[1], qkv[2]
        phi = lambda t: torch.where(t > 0, t + 1, torch.exp(t)) + 1e-6
        pq, pk = phi(q), phi(k)
        kv = torch.einsum("bhsd,bhse->bhde", pk, v)
        z = torch.einsum("bhsd->bhd", pk)
        num = torch.einsum("bhqd,bhde->bhqe", pq, kv)
        den = torch.einsum("bhqd,bhd->bhq", pq, z)[..., None] + 1e-6
        o = num / den
        return (lambda x, *d: x.permute(*d))(o, 0, 2, 1, 3).reshape(B, S, D) @ wo

    def _B_ref(u, A_re, A_im, B_re, B_im, C_re, C_im, log_dt, Dskip):
        Bsz, L, D = u.shape
        dt = torch.exp(log_dt)[:, None]
        A = A_re + 1j * A_im
        Bc = B_re + 1j * B_im
        Cc = C_re + 1j * C_im
        logdA = dt * A
        dB = dt * Bc
        l = (lambda x, dt: x.to(dt))(torch.arange(L), A_re.dtype)
        P = torch.exp(logdA[:, :, None] * l[None, None, :])
        K = torch.einsum("dn,dnl,dn->dl", Cc, P, dB).real
        L2 = 2 * L
        Kp = _pad(K, ((0, 0), (0, L)))
        up = (lambda x, *d: x.permute(*d))(u, 0, 2, 1)
        up = _pad(up, ((0, 0), (0, 0), (0, L)))
        Kf = (lambda x, axes=(-2,-1): torch.fft.rfft2(x.float(), dim=axes))(Kp[:, None, :], axes=(-2, -1))
        Uf = (lambda x, axes=(-2,-1): torch.fft.rfft2(x.float(), dim=axes))(up[:, :, None, :], axes=(-2, -1))
        Yf = Uf * Kf[None, :, :, :]
        y = (lambda x, s, axes=(-2,-1): torch.fft.irfft2(x, s=s, dim=axes).to(torch.bfloat16))(Yf, (1, L2), axes=(-2, -1))[:, :, 0, :L]
        y = (lambda x, *d: x.permute(*d))(y, 0, 2, 1) + u * Dskip[None, None, :]
        return y

    x = A_x
    g = _sigmoid((x * gate_w).sum(-1, keepdims=True))
    a = _A_ref(x, A_wqkv, A_wo)
    b = _B_ref(x, B_A_re, B_A_im, B_B_re, B_B_im, B_C_re, B_C_im, B_log_dt, B_Dskip)
    return g * a + (1 - g) * b
```

```cpp
#include <hip/hip_runtime.h>
#include <hip/hip_bf16.h>
#include <stdint.h>

typedef unsigned short u16;
__device__ __forceinline__ float bf2f(u16 v) { return __uint_as_float(((unsigned)v) << 16); }
__device__ __forceinline__ u16 f2bf(float f) { __hip_bfloat16 b = __float2bfloat16(f); return *reinterpret_cast<u16*>(&b); }
__device__ __forceinline__ float rbf(float f) { return bf2f(f2bf(f)); }

constexpr int S = 2048, D = 576, H = 32, DH = 18, N = 128;

__global__ void k_qkv(const u16* x, const u16* w, u16* qkv) {
  int n = blockIdx.x * 64 + threadIdx.x; int s = blockIdx.y;
  if (n >= 3 * D) return;
  float acc = 0.f;
  for (int k = 0; k < D; ++k) acc += bf2f(x[s * D + k]) * bf2f(w[k * 3 * D + n]);
  float t = rbf(acc);
  if (n < 2 * D) {
    float p = t > 0.f ? rbf(t + 1.f) : rbf(expf(t));
    t = rbf(p + 1e-6f);
  }
  qkv[s * 3 * D + n] = f2bf(t);
}
__global__ void k_kv(const u16* qkv, float* kv, float* z) {
  int h = blockIdx.x; int t = threadIdx.x;
  if (t >= DH * (DH + 1)) return;
  int d = t / (DH + 1), e = t % (DH + 1);
  float acc = 0.f;
  for (int s = 0; s < S; ++s) {
    float pk = bf2f(qkv[s * 3 * D + D + h * DH + d]);
    float v = e < DH ? bf2f(qkv[s * 3 * D + 2 * D + h * DH + e]) : 1.f;
    acc += pk * v;
  }
  if (e < DH) kv[(h * DH + d) * DH + e] = rbf(acc); else z[h * DH + d] = rbf(acc);
}
__global__ void k_o(const u16* qkv, const float* kv, const float* z, u16* o) {
  int c = blockIdx.x * 64 + threadIdx.x; int s = blockIdx.y;
  int h = c / DH, e = c % DH;
  float num = 0.f, den = 0.f;
  for (int d = 0; d < DH; ++d) {
    float pq = bf2f(qkv[s * 3 * D + h * DH + d]);
    num += pq * kv[(h * DH + d) * DH + e];
    den += pq * z[h * DH + d];
  }
  num = rbf(num); den = rbf(rbf(den) + 1e-6f);
  o[s * D + c] = f2bf(num / den);
}
__global__ void k_kgen(const u16* Are, const u16* Aim, const u16* Bre, const u16* Bim, const u16* Cre, const u16* Cim, const u16* logdt, float* K) {
  int l = blockIdx.x * 64 + threadIdx.x; int d = blockIdx.y;
  float dt = rbf(expf(bf2f(logdt[d])));
  float lr = (float)l;
  float acc = 0.f;
  for (int n = 0; n < N; ++n) {
    float ar = dt * bf2f(Are[d * N + n]), ai = dt * bf2f(Aim[d * N + n]);
    float br = dt * bf2f(Bre[d * N + n]), bi = dt * bf2f(Bim[d * N + n]);
    float cr = bf2f(Cre[d * N + n]), ci = bf2f(Cim[d * N + n]);
    float cbr = cr * br - ci * bi, cbi = cr * bi + ci * br;
    float e = expf(ar * lr); float s_, c_; sincosf(ai * lr, &s_, &c_);
    acc += e * (cbr * c_ - cbi * s_);
  }
  K[d * S + l] = acc;
}
__global__ void k_conv(const u16* x, const float* K, u16* y) {
  int d = blockIdx.x * 64 + threadIdx.x; int l = blockIdx.y;
  float acc = 0.f;
  for (int j = 0; j <= l; ++j) acc += K[d * S + j] * bf2f(x[(l - j) * D + d]);
  y[l * D + d] = f2bf(acc);
}
__global__ void k_final(const u16* x, const u16* o, const u16* wo, const u16* y, const u16* Dskip, const u16* gw, u16* out) {
  int c = blockIdx.x * 64 + threadIdx.x; int s = blockIdx.y;
  float gs = 0.f;
  for (int k = 0; k < D; ++k) gs += rbf(bf2f(x[s * D + k]) * bf2f(gw[k]));
  gs = rbf(gs);
  float g = rbf(1.f / (1.f + expf(-gs)));
  float acc = 0.f;
  for (int k = 0; k < D; ++k) acc += bf2f(o[s * D + k]) * bf2f(wo[k * D + c]);
  float a = rbf(acc);
  float b = rbf(bf2f(y[s * D + c]) + rbf(bf2f(x[s * D + c]) * bf2f(Dskip[c])));
  float r = rbf(g * a) + rbf(rbf(1.f - g) * b);
  out[s * D + c] = f2bf(r);
}

extern "C" void kernel_launch(void* const* d_in, const int* in_sizes, int n_in,
                              void* d_out, int out_size, void* d_ws, size_t ws_size,
                              hipStream_t stream) {
  const u16* x = (const u16*)d_in[0]; const u16* wqkv = (const u16*)d_in[1]; const u16* wo = (const u16*)d_in[2];
  const u16* Are = (const u16*)d_in[4]; const u16* Aim = (const u16*)d_in[5];
  const u16* Bre = (const u16*)d_in[6]; const u16* Bim = (const u16*)d_in[7];
  const u16* Cre = (const u16*)d_in[8]; const u16* Cim = (const u16*)d_in[9];
  const u16* logdt = (const u16*)d_in[10]; const u16* Dskip = (const u16*)d_in[11]; const u16* gw = (const u16*)d_in[12];
  char* ws = (char*)d_ws;
  u16* qkv = (u16*)ws; ws += S * 3 * D * 2;
  float* kv = (float*)ws; ws += H * DH * DH * 4;
  float* z = (float*)ws; ws += H * DH * 4;
  u16* o = (u16*)ws; ws += S * D * 2;
  float* K = (float*)ws; ws += D * S * 4;
  u16* y = (u16*)ws; ws += S * D * 2;
  k_qkv<<<dim3(27, S), 64, 0, stream>>>(x, wqkv, qkv);
  k_kv<<<H, 384, 0, stream>>>(qkv, kv, z);
  k_o<<<dim3(9, S), 64, 0, stream>>>(qkv, kv, z, o);
  k_kgen<<<dim3(S / 64, D), 64, 0, stream>>>(Are, Aim, Bre, Bim, Cre, Cim, logdt, K);
  k_conv<<<dim3(9, S), 64, 0, stream>>>(x, K, y);
  k_final<<<dim3(9, S), 64, 0, stream>>>(x, o, wo, y, Dskip, gw, (u16*)d_out);
}
```

```cpp
#include <hip/hip_runtime.h>
#include <stdint.h>

typedef unsigned short u16;
typedef unsigned int u32;
typedef __attribute__((ext_vector_type(8))) __bf16 bf16x8;
typedef __attribute__((ext_vector_type(2))) __bf16 bf16x2;
typedef __attribute__((ext_vector_type(4))) float f32x4;
typedef __attribute__((ext_vector_type(2))) float f32x2;
typedef __attribute__((ext_vector_type(4))) u32 u32x4;
typedef __attribute__((ext_vector_type(2))) u32 u32x2;
typedef __attribute__((ext_vector_type(4))) short s16x4;
typedef __attribute__((address_space(3))) char lds_char;
typedef __attribute__((address_space(3))) s16x4 lds_s16x4;
typedef __attribute__((address_space(3))) u32x4 lds_u32x4;
typedef __attribute__((address_space(3))) u32x2 lds_u32x2;
typedef __attribute__((address_space(3))) u32 lds_u32;
typedef __attribute__((address_space(3))) u16 lds_u16;
typedef __attribute__((address_space(3))) float lds_f32;
typedef __attribute__((address_space(3))) f32x4 lds_f32x4;
typedef __attribute__((address_space(3))) f32x2 lds_f32x2;
typedef __attribute__((address_space(1))) const u32 g_cu32;
struct __attribute__((packed, aligned(2))) u32x4_u { u32x4 v; };

constexpr int S = 2048, D = 576, N3 = 1728;
constexpr size_t OFF_PQ = 0;
constexpr size_t OFF_O = OFF_PQ + 128 * 18432;
constexpr size_t OFF_KVP = OFF_O + 128 * 18432;
constexpr size_t OFF_XT = OFF_KVP + 32 * 16 * 1520;
constexpr size_t OFF_IMG = OFF_XT + 576 * 4096;
constexpr size_t OFF_BT = OFF_IMG + 576 * 2048 + 256;
constexpr size_t OFF_GP = OFF_BT + 576 * 4096;
constexpr size_t OFF_END = OFF_GP + 2 * 2048 * 4;

#define STAMP(k, s) do {} while (0)

__device__ __forceinline__ u32 pk2(float a, float b) {
  f32x2 v = {a, b};
  bf16x2 r = __builtin_convertvector(v, bf16x2);
  return __builtin_bit_cast(u32, r);
}
__device__ __forceinline__ float lo16(u32 v) { return __uint_as_float(v << 16); }
__device__ __forceinline__ float hi16(u32 v) { return __uint_as_float(v & 0xffff0000u); }
__device__ __forceinline__ f32x4 mfma(u32x4 a, u32x4 b, f32x4 c) {
  return __builtin_amdgcn_mfma_f32_16x16x32_bf16(__builtin_bit_cast(bf16x8, a), __builtin_bit_cast(bf16x8, b), c, 0, 0, 0);
}
__device__ __forceinline__ u32x2 trread(lds_char* p) {
  s16x4 v = __builtin_amdgcn_ds_read_tr16_b64_v4i16((lds_s16x4*)p);
  return __builtin_bit_cast(u32x2, v);
}
__device__ __forceinline__ u32x4 cat(u32x2 a, u32x2 b) { return (u32x4){a.x, a.y, b.x, b.y}; }
__device__ __forceinline__ f32x2 cmul(f32x2 a, f32x2 b) {
  f32x2 t = {a.x * b.x, a.x * b.y};
  f32x2 s = {-b.y, b.x};
  f32x2 ay = {a.y, a.y};
  return __builtin_elementwise_fma(ay, s, t);
}
__device__ __forceinline__ u32 phi2(u32 t2) {
  float t0 = lo16(t2), t1 = hi16(t2);
  float e0 = __builtin_amdgcn_exp2f(t0 * 1.44269504088896f), e1 = __builtin_amdgcn_exp2f(t1 * 1.44269504088896f);
  float p0 = t0 > 0.f ? t0 + 1.f : e0, p1 = t1 > 0.f ? t1 + 1.f : e1;
  u32 p = pk2(p0, p1);
  return pk2(lo16(p) + 1e-6f, hi16(p) + 1e-6f);
}
#define BARRIER() asm volatile("s_barrier" ::: "memory")
#define WAITVM(n) asm volatile("s_waitcnt vmcnt(" #n ")" ::: "memory")
#define WAITLG0() asm volatile("s_waitcnt lgkmcnt(0)" ::: "memory")

constexpr int SLOT = 34816;
constexpr int K1_LDS = 156672;
constexpr int TROW = 544;

template <bool ISQ>
__device__ __forceinline__ void g1_main(lds_char* sm, int w, int lane, f32x4 (&acc)[2][9]) {
  const int r = lane & 15, g = lane >> 4;
  const int sw = (r >> 1) & 7;
  int aoff0 = (32 * w + r) * 128 + ((g ^ sw) << 4);
  int aoff1 = (32 * w + r) * 128 + (((4 + g) ^ sw) << 4);
  const int q = (lane >> 2) & 3, p = lane & 3;
  const int rho = 16 * (g >> 1) + 4 * (g & 1) + q;
  int boff = 16384 + rho * 288 + 8 * p;
  int moff = 16384 + rho * 288 + 72 * p + 32;
  u32x4 A[2][2];
  u32x4 B[6];
  auto rdA = [&](int kk, int base) {
    int a = (kk ? aoff1 : aoff0) + base;
    A[kk][0] = *(lds_u32x4*)(sm + a);
    A[kk][1] = *(lds_u32x4*)(sm + a + 2048);
  };
  auto rdB = [&](int u, int base) {
    int kk = u / 9, t = u % 9;
    int a = (t == 8 ? moff : boff + 72 * (t >> 1) + 40 * (t & 1)) + base + kk * 32 * 288;
    B[u % 6] = cat(trread(sm + a), trread(sm + a + 8 * 288));
  };
  BARRIER();
  rdA(0, 0);
#pragma unroll
  for (int u = 0; u < 5; ++u) rdB(u, 0);
  int cur = 0;
  for (int st = 0; st < 9; ++st) {
    int nxt = (st & 3) == 3 ? 0 : cur + SLOT;
#pragma unroll
    for (int u = 0; u < 18; ++u) {
      if (u == 13) { if (st < 8) BARRIER(); }
      if (u == 4) rdA(1, cur);
      if (u == 13) rdA(0, nxt);
      if (u + 5 < 18) rdB(u + 5, cur); else rdB(u + 5 - 18, nxt);
      const int kk = u / 9, t = u % 9;
      if (ISQ) {
        acc[0][t] = mfma(B[u % 6], A[kk][0], acc[0][t]);
        acc[1][t] = mfma(B[u % 6], A[kk][1], acc[1][t]);
      } else {
        acc[0][t] = mfma(A[kk][0], B[u % 6], acc[0][t]);
        acc[1][t] = mfma(A[kk][1], B[u % 6], acc[1][t]);
      }
      __builtin_amdgcn_sched_barrier(0);
    }
    cur = nxt;
  }
}

__global__ __launch_bounds__(576, 1) void k1(const u16* __restrict__ x, const u16* __restrict__ wqkv,
                                             const u16* __restrict__ Are, const u16* __restrict__ Aim,
                                             const u16* __restrict__ Bre, const u16* __restrict__ Bim,
                                             const u16* __restrict__ Cre, const u16* __restrict__ Cim,
                                             const u16* __restrict__ logdt, const u16* __restrict__ gw,
                                             char* __restrict__ ws) {
  extern __shared__ __attribute__((aligned(16))) char shm_[];
  lds_char* sm = (lds_char*)shm_;
  const int tid = threadIdx.x, lane = tid & 63;
  const int w = __builtin_amdgcn_readfirstlane(tid >> 6);
  const int xcd = blockIdx.x & 7, slot = blockIdx.x >> 3;
  if (w == 0) STAMP(0, 0);
  if (slot < 24) {
    const int rb = (xcd >> 1) * 4 + slot / 6;
    const int cj = slot % 6;
    const bool isq = cj < 2;
    const int qm = (xcd & 1) * 2 + cj;
    const int kvj = (xcd & 1) * 4 + cj - 2;
    if (w == 8) return;
    if (w >= 4) {
      const int lw = w - 4;
      const char* pa[4];
      const char* pb[5];
#pragma unroll
      for (int j = 0; j < 4; ++j) {
        int row = (lw * 4 + j) * 8 + (lane >> 3);
        int c = (lane & 7) ^ ((row >> 1) & 7);
        pa[j] = (const char*)x + (size_t)(rb * 128 + row) * (D * 2) + c * 16;
      }
#pragma unroll
      for (int j = 0; j < 5; ++j) {
        int idx = (lw + 4 * j) * 64 + lane;
        int rho = idx / 18, ch = idx % 18;
        int kr = (rho & ~12) | ((rho & 4) << 1) | ((rho & 8) >> 1);
        int cb = isq ? qm * 288 + ch * 16 : (ch < 9 ? 1152 + kvj * 144 + ch * 16 : 2304 + kvj * 144 + (ch - 9) * 16);
        pb[j] = (const char*)wqkv + (size_t)kr * (N3 * 2) + cb;
      }
      auto issue = [&](int st) {
        lds_char* base = sm + (st & 3) * SLOT;
#pragma unroll
        for (int j = 0; j < 4; ++j)
          __builtin_amdgcn_global_load_lds((g_cu32*)(pa[j] + st * 128), (lds_u32*)(base + (lw * 4 + j) * 1024), 16, 0, 0);
#pragma unroll
        for (int j = 0; j < 5; ++j)
          if (j < 4 || lw < 2)
            __builtin_amdgcn_global_load_lds((g_cu32*)(pb[j] + (size_t)st * 64 * N3 * 2), (lds_u32*)(base + 16384 + (lw + 4 * j) * 1024), 16, 0, 0);
      };
      issue(0); issue(1); issue(2); issue(3);
      if (lw < 2) {
        WAITVM(27); BARRIER();
        WAITVM(18); BARRIER();
        WAITVM(9); BARRIER(); issue(4);
        WAITVM(9); BARRIER(); issue(5);
        WAITVM(9); BARRIER(); issue(6);
        WAITVM(9); BARRIER(); issue(7);
        WAITVM(9); BARRIER(); issue(8);
        WAITVM(9); BARRIER();
        WAITVM(0); BARRIER();
      } else {
        WAITVM(24); BARRIER();
        WAITVM(16); BARRIER();
        WAITVM(8); BARRIER(); issue(4);
        WAITVM(8); BARRIER(); issue(5);
        WAITVM(8); BARRIER(); issue(6);
        WAITVM(8); BARRIER(); issue(7);
        WAITVM(8); BARRIER(); issue(8);
        WAITVM(8); BARRIER();
        WAITVM(0); BARRIER();
      }
      if (isq) return;
    } else {
      f32x4 acc[2][9];
#pragma unroll
      for (int i = 0; i < 2; ++i)
#pragma unroll
        for (int t = 0; t < 9; ++t) acc[i][t] = (f32x4){0.f, 0.f, 0.f, 0.f};
      if (isq) {
        g1_main<true>(sm, w, lane, acc);
        if (w == 0) STAMP(0, 1);
#pragma unroll
        for (int i = 0; i < 2; ++i) {
          char* dst = ws + OFF_PQ + (size_t)(rb * 8 + w * 2 + i) * 18432;
#pragma unroll
          for (int pp = 0; pp < 4; ++pp) {
            u32x4 v = {pk2(acc[i][2 * pp][0], acc[i][2 * pp][1]), pk2(acc[i][2 * pp][2], acc[i][2 * pp][3]),
                       pk2(acc[i][2 * pp + 1][0], acc[i][2 * pp + 1][1]), pk2(acc[i][2 * pp + 1][2], acc[i][2 * pp + 1][3])};
            *(u32x4*)(dst + (qm * 4 + pp) * 1024 + lane * 16) = v;
          }
          u32x2 m = {pk2(acc[i][8][0], acc[i][8][1]), pk2(acc[i][8][2], acc[i][8][3])};
          *(u32x2*)(dst + 16384 + qm * 512 + lane * 8) = m;
        }
        if (w == 0) STAMP(0, 2);
        return;
      }
      g1_main<false>(sm, w, lane, acc);
      if (w == 0) STAMP(0, 1);
      u32x4 F[9];
#pragma unroll
      for (int t = 0; t < 9; ++t) {
        F[t] = (u32x4){pk2(acc[0][t][0], acc[0][t][1]), pk2(acc[0][t][2], acc[0][t][3]),
                       pk2(acc[1][t][0], acc[1][t][1]), pk2(acc[1][t][2], acc[1][t][3])};
      }
      u32x4 Mk, Mv;
      const bool isk = (lane & 15) < 8;
#pragma unroll
      for (int i = 0; i < 4; ++i) {
#pragma unroll
        for (int t = 0; t < 4; ++t) F[t][i] = phi2(F[t][i]);
        Mk[i] = phi2(F[8][i]);
        Mv[i] = isk ? 0x3f803f80u : F[8][i];
      }
      const f32x4 z4 = {0.f, 0.f, 0.f, 0.f};
      lds_char* pbuf = sm + SLOT + w * 6080;
      const int c = lane & 15, g = lane >> 4;
#pragma unroll
      for (int h = 0; h < 4; ++h) {
        f32x4 t1 = mfma(F[h], F[4 + h], z4);
        f32x4 t2 = mfma(F[h], Mv, z4);
        f32x4 t3 = mfma(Mk, F[4 + h], z4);
        lds_char* hb = pbuf + h * 1520;
        *(lds_f32x4*)(hb + c * 80 + g * 16) = t1;
        if (c == 0 || (c >> 1) == 4 + h) *(lds_f32x4*)(hb + (c == 0 ? 18 : 16 + (c & 1)) * 80 + g * 16) = t2;
        if (g == (h >> 1)) *(lds_f32x2*)(hb + c * 80 + 64) = (h & 1) ? (f32x2){t3[2], t3[3]} : (f32x2){t3[0], t3[1]};
      }
      {
        f32x4 t4 = mfma(Mk, Mv, z4);
        if (g < 2) {
          if (c == 0) {
            *(lds_f32x2*)(pbuf + (2 * g) * 1520 + 18 * 80 + 64) = (f32x2){t4[0], t4[1]};
            *(lds_f32x2*)(pbuf + (2 * g + 1) * 1520 + 18 * 80 + 64) = (f32x2){t4[2], t4[3]};
          } else if ((c >> 2) == 2 + g) {
            int par = (c >> 1) & 1;
            *(lds_f32x2*)(pbuf + (2 * g + par) * 1520 + (16 + (c & 1)) * 80 + 64) = par ? (f32x2){t4[2], t4[3]} : (f32x2){t4[0], t4[1]};
          }
        }
      }
      WAITLG0();
    }
    BARRIER();
    if (tid < 380) {
      lds_char* pb0 = sm + SLOT + tid * 16;
      f32x4 s = *(lds_f32x4*)(pb0) + *(lds_f32x4*)(pb0 + 6080);
      s += *(lds_f32x4*)(pb0 + 2 * 6080) + *(lds_f32x4*)(pb0 + 3 * 6080);
      int h = tid / 95, ch = tid % 95;
      *(f32x4*)(ws + OFF_KVP + (size_t)((kvj * 4 + h) * 16 + rb) * 1520 + ch * 16) = s;
    }
    if (w == 0) STAMP(0, 2);
    return;
  }
  const int ps = slot - 24;
  const int d = 72 * xcd + 9 * ps + w;
  const u32 vAre = *(const u32*)(Are + d * 128 + 2 * lane), vAim = *(const u32*)(Aim + d * 128 + 2 * lane);
  const u32 vBre = *(const u32*)(Bre + d * 128 + 2 * lane), vBim = *(const u32*)(Bim + d * 128 + 2 * lane);
  const u32 vCre = *(const u32*)(Cre + d * 128 + 2 * lane), vCim = *(const u32*)(Cim + d * 128 + 2 * lane);
  const u16 vld = logdt[d];
  const int row0 = 512 * (xcd >> 1) + 64 * ps, col0 = 288 * (xcd & 1);
  const int xr = tid / 36, xc = tid % 36;
  u32x4 xv[4];
#pragma unroll
  for (int i = 0; i < 4; ++i) xv[i] = *(const u32x4*)(x + (size_t)(row0 + xr + 16 * i) * D + col0 + xc * 8);
  const u32x4 gv = *(const u32x4*)(gw + col0 + xc * 8);
  {
    lds_char* tb = sm + w * 17408;
    lds_char* tV = tb;
    lds_char* tU = tb + 16 * TROW;
    const float dt = lo16(pk2(__builtin_amdgcn_exp2f(lo16(vld) * 1.44269504088896f), 0.f));
    f32x2 w1[2], w16[2], w256[2], cb[2];
#pragma unroll
    for (int m = 0; m < 2; ++m) {
      float ar = dt * (m ? hi16(vAre) : lo16(vAre)), ai = dt * (m ? hi16(vAim) : lo16(vAim));
      float br = dt * (m ? hi16(vBre) : lo16(vBre)), bi = dt * (m ? hi16(vBim) : lo16(vBim));
      float cr = m ? hi16(vCre) : lo16(vCre), ci = m ? hi16(vCim) : lo16(vCim);
      cb[m] = (f32x2){cr * br - ci * bi, -(cr * bi + ci * br)};
      float le = ar * 1.44269504088896f, rv = ai * 0.15915494309189535f;
      float e1 = __builtin_amdgcn_exp2f(le), e16 = __builtin_amdgcn_exp2f(le * 16.f), e256 = __builtin_amdgcn_exp2f(le * 256.f);
      float r1 = __builtin_amdgcn_fractf(rv), r16 = __builtin_amdgcn_fractf(rv * 16.f), r256 = __builtin_amdgcn_fractf(rv * 256.f);
      w1[m] = (f32x2){e1 * __builtin_amdgcn_cosf(r1), -e1 * __builtin_amdgcn_sinf(r1)};
      w16[m] = (f32x2){e16 * __builtin_amdgcn_cosf(r16), e16 * __builtin_amdgcn_sinf(r16)};
      w256[m] = (f32x2){e256 * __builtin_amdgcn_cosf(r256), -e256 * __builtin_amdgcn_sinf(r256)};
    }
    auto put = [&](lds_char* t, int row, f32x2 a, f32x2 b) {
      *(lds_u32x2*)(t + row * TROW + lane * 8) = (u32x2){pk2(a.x, a.y), pk2(b.x, b.y)};
    };
    {
      f32x2 v0 = w16[0], v1 = w16[1], u0 = cb[0], u1 = cb[1];
      *(lds_u32x2*)(tV + lane * 8) = (u32x2){0x00003f80u, 0x00003f80u};
      put(tV, 1, v0, v1);
      put(tU, 0, u0, u1);
#pragma unroll
      for (int e = 1; e < 16; ++e) {
        u0 = cmul(u0, w1[0]); u1 = cmul(u1, w1[1]);
        put(tU, e, u0, u1);
        if (e >= 2) { v0 = cmul(v0, w16[0]); v1 = cmul(v1, w16[1]); put(tV, e, v0, v1); }
      }
    }
    const int fr = (lane & 15) * TROW + (lane >> 4) * 16;
    u32x4 Vf[8];
    f32x4 t0 = {0.f, 0.f, 0.f, 0.f}, ta = t0, tbb = t0;
#pragma unroll
    for (int kk = 0; kk < 8; ++kk) {
      Vf[kk] = *(lds_u32x4*)(tV + fr + kk * 64);
      u32x4 uf = *(lds_u32x4*)(tU + fr + kk * 64);
      t0 = mfma(uf, Vf[kk], t0);
    }
    {
      f32x2 w2[2] = {cmul(w1[0], w1[0]), cmul(w1[1], w1[1])};
      f32x2 u0 = cmul(cb[0], w256[0]), u1 = cmul(cb[1], w256[1]);
      put(tV, 8, u0, u1);
#pragma unroll
      for (int i = 0; i < 8; ++i) {
        u0 = cmul(u0, w2[0]); u1 = cmul(u1, w2[1]);
        put(tV, i, u0, u1);
        if (i & 1) {
          const int o = i >> 1;
          f32x2 a0 = cmul(u0, w256[0]), a1 = cmul(u1, w256[1]);
          put(tU, o, a0, a1);
          a0 = cmul(a0, w256[0]); a1 = cmul(a1, w256[1]);
          put(tU, 4 + o, a0, a1);
          if (o & 1) {
            const int o3 = o >> 1;
#pragma unroll
            for (int qq = 0; qq < 4; ++qq) {
              a0 = cmul(a0, w256[0]); a1 = cmul(a1, w256[1]);
              put(tU, 8 + 2 * qq + o3, a0, a1);
            }
          }
        }
      }
    }
#pragma unroll
    for (int kk = 0; kk < 8; ++kk) {
      u32x4 af = *(lds_u32x4*)(tV + fr + kk * 64);
      u32x4 bf = *(lds_u32x4*)(tU + fr + kk * 64);
      ta = mfma(af, Vf[kk], ta);
      tbb = mfma(Vf[kk], bf, tbb);
    }
    const u32x4 zz = {0u, 0u, 0u, 0u};
    *(lds_u32x4*)(tb + lane * 16) = zz;
    *(lds_u32x4*)(tb + 1024 + lane * 16) = zz;
    const int c = lane & 15, g = lane >> 4;
    *(lds_u32x2*)(tb + 2 * (540 - 16 * c - 4 * g)) = (u32x2){pk2(t0[3], t0[2]), pk2(t0[1], t0[0])};
    if (g < 2) {
      lds_u32* pR = (lds_u32*)(tb + 2 * (278 - 16 * c - 8 * g));
      pR[0] = pk2(ta[3], ta[3]); pR[1] = pk2(ta[3], ta[2]); pR[2] = pk2(ta[1], ta[1]); pR[3] = pk2(ta[1], ta[0]);
      if (lane == 31) *(lds_u16*)(tb + 2 * 29) = (u16)pk2(ta[3], ta[3]);
    } else if (lane == 32) {
      *(lds_u32*)(tb + 2 * 286) = pk2(ta[0], ta[0]);
    }
    {
      int p0 = c < 8 ? 560 + 64 * (c & 3) + 44 - 16 * (c >> 2) - 4 * g : 816 + 80 * (c & 1) + 60 - 16 * ((c - 8) >> 1) - 4 * g;
      *(lds_u32x2*)(tb + 2 * p0) = (u32x2){pk2(tbb[3], tbb[2]), pk2(tbb[1], tbb[0])};
    }
    u32x4 i0 = *(lds_u32x4*)(tb + lane * 16), i1 = *(lds_u32x4*)(tb + 1024 + lane * 16);
    *(u32x4*)(ws + OFF_IMG + (size_t)d * 2048 + lane * 16) = i0;
    *(u32x4*)(ws + OFF_IMG + (size_t)d * 2048 + 1024 + lane * 16) = i1;
  }
  if (w == 0) STAMP(0, 3);
  __syncthreads();
  constexpr int XROW = 592;
  lds_char* gpart = sm + 64 * XROW;
#pragma unroll
  for (int i = 0; i < 4; ++i) {
    *(lds_u32x4*)(sm + (xr + 16 * i) * XROW + xc * 16) = xv[i];
    float s = 0.f;
#pragma unroll
    for (int j = 0; j < 4; ++j) {
      u32 pr = pk2(lo16(xv[i][j]) * lo16(gv[j]), hi16(xv[i][j]) * hi16(gv[j]));
      s += lo16(pr) + hi16(pr);
    }
    *(lds_f32*)(gpart + ((xr + 16 * i) * 36 + xc) * 4) = s;
  }
  __syncthreads();
  {
    const int g = lane >> 4, q = (lane >> 2) & 3, p = lane & 3;
#pragma unroll
    for (int j = 0; j < 4; ++j) {
      const int ct = 2 * w + (j >> 1), half = j & 1;
      lds_char* a = sm + (32 * half + 8 * g + q) * XROW + ct * 32 + p * 8;
      u32x4 v = cat(trread(a), trread(a + 4 * XROW));
      *(u32x4*)(ws + OFF_XT + (size_t)(col0 + 16 * ct + (lane & 15)) * 4096 + (row0 + 32 * half + 8 * g) * 2) = v;
    }
    if (tid < 256) {
      lds_f32* gp = (lds_f32*)gpart + (tid >> 2) * 36 + (tid & 3) * 9;
      float s = 0.f;
#pragma unroll
      for (int j = 0; j < 9; ++j) s += gp[j];
      s += __shfl_xor(s, 1);
      s += __shfl_xor(s, 2);
      if ((tid & 3) == 0) ((float*)(ws + OFF_GP))[(xcd & 1) * 2048 + row0 + (tid >> 2)] = s;
    }
  }
  if (w == 0) STAMP(0, 4);
}

constexpr int CW = 8832;
constexpr int K2_LDS = 4 * CW;

__device__ __forceinline__ u32x4 band(int b, int g, int off, int len, int sh) {
  u32x4 r;
#pragma unroll
  for (int i = 0; i < 4; ++i) {
    int c0 = 8 - 8 * g + 2 * i - sh - (b - off - len + 1);
    u32 v = ((unsigned)c0 < (unsigned)len ? 0x3f80u : 0u) | ((unsigned)(c0 + 1) < (unsigned)len ? 0x3f800000u : 0u);
    r[i] = v;
  }
  return r;
}

__global__ __launch_bounds__(256, 2) void k2(const u16* __restrict__ dskip, char* __restrict__ ws) {
  extern __shared__ __attribute__((aligned(16))) char shm_[];
  lds_char* sm = (lds_char*)shm_;
  const int tid = threadIdx.x, lane = tid & 63;
  const int w = __builtin_amdgcn_readfirstlane(tid >> 6);
  const int xcd = blockIdx.x & 7, slot = blockIdx.x >> 3;
  const int c = lane & 15, g = lane >> 4;
  const f32x4 z4 = {0.f, 0.f, 0.f, 0.f};
  if (w == 0) STAMP(1, 0);
  if (slot < 18) {
    const int d = 72 * xcd + 4 * slot + w;
    lds_char* su = sm + w * CW;
    lds_char* si = su + 4608;
    const char* gu = ws + OFF_XT + (size_t)d * 4096 + lane * 16;
    const char* gi = ws + OFF_IMG + (size_t)d * 2048 + lane * 16;
    u32x4 lu[4], li[4];
    li[0] = *(const u32x4*)(gi); li[1] = *(const u32x4*)(gi + 1024);
    li[2] = ((const u32x4_u*)(gi + 2))->v; li[3] = ((const u32x4_u*)(gi + 1026))->v;
#pragma unroll
    for (int i = 0; i < 4; ++i) lu[i] = *(const u32x4*)(gu + i * 1024);
    const float dsk = lo16(dskip[d]);
    u32x4 bd[6];
    bd[0] = band(c, g, 3, 3, 0); bd[1] = band(c, g, 6, 5, 0); bd[2] = band(c, g, 11, 3, 0); bd[3] = band(c, g, 14, 5, 8);
    bd[4] = band(c, g, 5, 7, 0); bd[5] = band(c, g, 12, 9, 8);
    u32x4 fixm;
#pragma unroll
    for (int i = 0; i < 4; ++i) {
      int l0 = c + 8 * g - 2 * i - 11;
      fixm[i] = ((unsigned)l0 < 2u ? 0xffffu : 0u) | ((unsigned)(l0 - 1) < 2u ? 0xffff0000u : 0u);
    }
    if (lane < 30) *(lds_u32x4*)(su + lane * 16) = (u32x4){0u, 0u, 0u, 0u};
    *(lds_u32x4*)(si + lane * 16) = li[0]; *(lds_u32x4*)(si + 1024 + lane * 16) = li[1];
    *(lds_u32x4*)(si + 2112 + lane * 16) = li[2]; *(lds_u32x4*)(si + 2112 + 1024 + lane * 16) = li[3];
#pragma unroll
    for (int i = 0; i < 4; ++i) *(lds_u32x4*)(su + 480 + i * 1024 + lane * 16) = lu[i];
    if (w == 0) STAMP(1, 1);
    lds_char* ub = su + 480 + 32 * c + 16 - 16 * g;
    lds_char* kb = si + ((c & 1) ? 2 * (551 - 8 * g - c) : 2112 + 2 * (550 - 8 * g - c));
    lds_char* sb = si + ((c & 1) ? 2 * (79 - c + 4 * g) : 2112 + 2 * (78 - c + 4 * g));
    auto ufrag = [&](int s) { return *(lds_u32x4*)(ub + 64 * s); };
    auto kfrag = [&](int i) { lds_u32* p = (lds_u32*)(kb - 64 * i); return (u32x4){p[0], p[1], p[2], p[3]}; };
    auto sfrag = [&](int e0, int e1) { lds_u32* p = (lds_u32*)(sb + 2 * e0); lds_u32* q = (lds_u32*)(sb + 2 * e1); return (u32x4){p[0], p[1], q[0], q[1]}; };
    f32x4 acc[8];
    {
      u32x4 U[8], F2[6][2], F3[4];
#pragma unroll
      for (int m = 0; m < 8; ++m) U[m] = ufrag(8 * m);
      u32x4 A0 = kfrag(0), A1 = kfrag(8), A2 = kfrag(16);
      u32 k1024 = *(lds_u16*)(si + 2 * (560 + 192 + 16));
      k1024 |= k1024 << 16;
#pragma unroll
      for (int T = 0; T < 8; ++T) acc[T] = mfma(A0, U[T], z4);
#pragma unroll
      for (int m = 0; m < 6; ++m) {
        u32x4 Us = *(lds_u32x4*)(ub + 64 * 8 * m - 16);
        f32x4 f0 = mfma(U[m], bd[0], z4), f1 = mfma(U[m], bd[1], z4), f2 = mfma(U[m], bd[2], z4), f3 = mfma(Us, bd[3], z4);
        F2[m][0] = (u32x4){pk2(f0[0], f0[1]), pk2(f0[2], f0[3]), pk2(f1[0], f1[1]), pk2(f1[2], f1[3])};
        F2[m][1] = (u32x4){pk2(f2[0], f2[1]), pk2(f2[2], f2[3]), pk2(f3[0], f3[1]), pk2(f3[2], f3[3])};
        if (m < 4) {
          f32x4 f4 = mfma(U[m], bd[4], z4), f5 = mfma(Us, bd[5], z4);
          F3[m] = (u32x4){pk2(f4[0], f4[1]), pk2(f4[2], f4[3]), pk2(f5[0], f5[1]), pk2(f5[2], f5[3])};
        }
      }
#pragma unroll
      for (int T = 1; T < 8; ++T) acc[T] = mfma(A1, U[T - 1], acc[T]);
#pragma unroll
      for (int T = 2; T < 8; ++T) acc[T] = mfma(A2, U[T - 2], acc[T]);
      u32x4 fx = {fixm[0] & k1024, fixm[1] & k1024, fixm[2] & k1024, fixm[3] & k1024};
#pragma unroll
      for (int T = 4; T < 8; ++T) acc[T] = mfma(fx, U[T - 4], acc[T]);
#pragma unroll
      for (int dl = 2; dl <= 4; ++dl)
#pragma unroll
        for (int pr = 0; pr < 2; ++pr) {
          u32x4 Sf = sfrag(560 + 128 * pr - 16 * dl, 560 + 128 * pr + 64 - 16 * dl);
#pragma unroll
          for (int T = dl; T < 8; ++T) acc[T] = mfma(F2[T - dl][pr], Sf, acc[T]);
        }
#pragma unroll
      for (int dl = 4; dl < 8; ++dl) {
        u32x4 Sf = sfrag(816 + 48 - 16 * dl, 816 + 80 + 48 - 16 * dl);
#pragma unroll
        for (int T = dl; T < 8; ++T) acc[T] = mfma(F3[T - dl], Sf, acc[T]);
      }
    }
#pragma unroll
    for (int r = 1; r < 8; ++r) {
      u32x4 A0 = kfrag(r), A1 = kfrag(8 + r);
      u32x4 Up;
#pragma unroll
      for (int T = 0; T < 8; ++T) {
        u32x4 U = ufrag(8 * T - r);
        if (T > 0) acc[T] = mfma(A1, Up, acc[T]);
        acc[T] = mfma(A0, U, acc[T]);
        Up = U;
      }
    }
    if (w == 0) STAMP(1, 2);
    char* gb = ws + OFF_BT + (size_t)d * 4096 + c * 32 + g * 8;
#pragma unroll
    for (int T = 0; T < 8; ++T) {
      u32x2 uu = *(lds_u32x2*)(su + 480 + 512 * T + 32 * c + 8 * g);
      u32 s0 = pk2(lo16(uu.x) * dsk, hi16(uu.x) * dsk), s1 = pk2(lo16(uu.y) * dsk, hi16(uu.y) * dsk);
      u32 y0 = pk2(acc[T][0], acc[T][1]), y1 = pk2(acc[T][2], acc[T][3]);
      u32x2 o = {pk2(lo16(y0) + lo16(s0), hi16(y0) + hi16(s0)), pk2(lo16(y1) + lo16(s1), hi16(y1) + hi16(s1))};
      *(u32x2*)(gb + 512 * T) = o;
    }
    if (w == 0) STAMP(1, 3);
    return;
  }
  const int os = slot - 18;
  const int pair = 8 * (xcd & 1) + (os & 7);
  const int rt0 = (2 * (xcd >> 1) + (os >> 3)) * 16 + w * 4;
  const int m = pair >> 2, gs = pair & 3;
  f32x4 part[16];
  const int par = tid >= 95 ? 1 : 0, ch = tid - 95 * par;
  if (tid < 190) {
    const char* kp = ws + OFF_KVP + (size_t)(2 * pair + par) * 16 * 1520 + ch * 16;
#pragma unroll
    for (int i = 0; i < 16; ++i) part[i] = *(const f32x4*)(kp + i * 1520);
  }
  u32x4 Q[4]; u32x2 QM[4];
#pragma unroll
  for (int i = 0; i < 4; ++i) {
    const char* src = ws + OFF_PQ + (size_t)(rt0 + i) * 18432;
    Q[i] = *(const u32x4*)(src + pair * 1024 + lane * 16);
    QM[i] = *(const u32x2*)(src + 16384 + m * 512 + (gs * 16 + c) * 8);
  }
  *(lds_u32x4*)(sm + tid * 16) = (u32x4){0u, 0u, 0u, 0u};
  __syncthreads();
  if (tid < 190) {
    f32x4 s = part[0];
#pragma unroll
    for (int i = 1; i < 16; ++i) s += part[i];
    const int e = ch / 5, cc = ch % 5;
    u32x2 v = {pk2(s[0], s[1]), pk2(s[2], s[3])};
    const int f = e < 16 ? par : 2 + par;
    const int row = e < 16 ? e : (e < 18 ? 4 * par + 2 + (e - 16) : par);
    lds_char* base = sm + f * 1024 + row * 16;
    const int nrep = e == 18 ? 4 : 1;
    for (int rep = 0; rep < nrep; ++rep) {
      if (cc < 4) *(lds_u32x2*)(base + rep * 64 + cc * 256) = v;
      else *(lds_u32*)(base + rep * 64 + 8 + 4 * par) = v.x;
    }
  }
#pragma unroll
  for (int i = 0; i < 4; ++i) {
#pragma unroll
    for (int j = 0; j < 4; ++j) Q[i][j] = phi2(Q[i][j]);
    QM[i][0] = phi2(QM[i][0]); QM[i][1] = phi2(QM[i][1]);
  }
  __syncthreads();
  if (w == 0) STAMP(1, 4);
  u32x4 Af[4];
#pragma unroll
  for (int f = 0; f < 4; ++f) Af[f] = *(lds_u32x4*)(sm + f * 1024 + lane * 16);
#pragma unroll
  for (int i = 0; i < 4; ++i) {
    u32x4 B1 = {Q[i][0], Q[i][1], QM[i][0], QM[i][1]}, B2 = {Q[i][2], Q[i][3], QM[i][0], QM[i][1]};
    f32x4 E = mfma(Af[0], B1, z4), O = mfma(Af[1], B2, z4);
    f32x4 X = mfma(Af[3], B2, mfma(Af[2], B1, z4));
    u32 dn = pk2(X[0], X[1]);
    dn = pk2(lo16(dn) + 1e-6f, hi16(dn) + 1e-6f);
    float re = __builtin_amdgcn_rcpf(lo16(dn)), ro = __builtin_amdgcn_rcpf(hi16(dn));
    u32 e0 = pk2(E[0], E[1]), e1 = pk2(E[2], E[3]), o0 = pk2(O[0], O[1]), o1 = pk2(O[2], O[3]), xx = pk2(X[2], X[3]);
    u32x4 ov = {pk2(lo16(e0) * re, hi16(e0) * re), pk2(lo16(e1) * re, hi16(e1) * re),
                pk2(lo16(o0) * ro, hi16(o0) * ro), pk2(lo16(o1) * ro, hi16(o1) * ro)};
    float rx = g == 0 ? re : ro;
    u32 xv = pk2(lo16(xx) * rx, hi16(xx) * rx);
    char* dst = ws + OFF_O + (size_t)(rt0 + i) * 18432;
    *(u32x4*)(dst + pair * 1024 + lane * 16) = ov;
    if (g < 2) *(u32*)(dst + 16384 + m * 512 + (gs * 16 + c) * 8 + 4 * g) = xv;
  }
  if (w == 0) STAMP(1, 5);
}

constexpr int K3_BT = 82944;
constexpr int K3_LDS = K3_BT + 72 * 128;

__global__ __launch_bounds__(512, 1) void k3(const u16* __restrict__ wo, char* __restrict__ ws, u16* __restrict__ out) {
  extern __shared__ __attribute__((aligned(16))) char shm_[];
  lds_char* sm = (lds_char*)shm_;
  const int tid = threadIdx.x, lane = tid & 63;
  const int w = __builtin_amdgcn_readfirstlane(tid >> 6);
  const int xcd = blockIdx.x & 7, slot = blockIdx.x >> 3;
  const int row0 = 256 * xcd + 64 * (slot >> 3), cg = slot & 7;
  if (w == 0) STAMP(2, 0);
  if (w >= 4) {
    const int lw = w - 4;
    const char* wb = (const char*)wo + cg * 144;
#pragma unroll
    for (int j = 0; j < 21; ++j) {
      int ins = lw + 4 * j;
      if (j < 20 || lw == 0) {
        int idx = ins * 64 + lane;
        int r = idx / 9, ch = idx % 9;
        __builtin_amdgcn_global_load_lds((g_cu32*)(wb + r * 1152 + ch * 16), (lds_u32*)(sm + ins * 1024), 16, 0, 0);
      }
    }
    if (lw >= 1) {
#pragma unroll
      for (int j = 0; j < 3; ++j) {
        int ins = (lw - 1) * 3 + j;
        int dd = ins * 8 + (lane >> 3);
        __builtin_amdgcn_global_load_lds((g_cu32*)(ws + OFF_BT + (size_t)(cg * 72 + dd) * 4096 + row0 * 2 + (lane & 7) * 16), (lds_u32*)(sm + K3_BT + ins * 1024), 16, 0, 0);
      }
      WAITVM(17); BARRIER();
      WAITVM(12); BARRIER();
      WAITVM(7); BARRIER();
      WAITVM(0); BARRIER();
    } else {
      WAITVM(15); BARRIER();
      WAITVM(10); BARRIER();
      WAITVM(5); BARRIER();
      WAITVM(0); BARRIER();
    }
    return;
  }
  const int c = lane & 15, g = lane >> 4, q = (lane >> 2) & 3, p = lane & 3;
  const char* osrc = ws + OFF_O + (size_t)(row0 / 16 + w) * 18432;
  u32x4 Of[18];
#pragma unroll
  for (int k = 0; k < 16; ++k) Of[k] = *(const u32x4*)(osrc + k * 1024 + lane * 16);
#pragma unroll
  for (int k = 0; k < 2; ++k)
    Of[16 + k] = cat(*(const u32x2*)(osrc + 16384 + (2 * k) * 512 + lane * 8), *(const u32x2*)(osrc + 16384 + (2 * k + 1) * 512 + lane * 8));
  const int srow = row0 + 16 * w + c;
  const float gs = ((const float*)(ws + OFF_GP))[srow] + ((const float*)(ws + OFF_GP))[2048 + srow];
  f32x4 acc[5];
#pragma unroll
  for (int t = 0; t < 5; ++t) acc[t] = (f32x4){0.f, 0.f, 0.f, 0.f};
  lds_char* ab = sm + (4 * g + q) * 144 + 8 * p;
  lds_char* ab4 = sm + (4 * g + q) * 144 + 128 + 8 * (p & 1);
  lds_char* mb = sm + (36 * g + 16 + q) * 144 + 8 * p;
  lds_char* mb4 = sm + (36 * g + 16 + q) * 144 + 128 + 8 * (p & 1);
#pragma unroll
  for (int ck = 0; ck < 4; ++ck) {
    BARRIER();
#pragma unroll
    for (int kk = 0; kk < 4; ++kk) {
      const int k = ck * 4 + kk;
#pragma unroll
      for (int t = 0; t < 5; ++t) {
        lds_char* a = (t < 4 ? ab + 32 * t : ab4) + k * 36 * 144;
        u32x4 Wf = cat(trread(a), trread(a + 20 * 144));
        acc[t] = mfma(Wf, Of[k], acc[t]);
      }
    }
    if (ck & 1) {
      const int k = ck >> 1;
#pragma unroll
      for (int t = 0; t < 5; ++t) {
        lds_char* a = (t < 4 ? mb + 32 * t : mb4) + k * 288 * 144;
        u32x4 Wf = cat(trread(a), trread(a + 144 * 144));
        acc[t] = mfma(Wf, Of[16 + k], acc[t]);
      }
    }
  }
  if (w == 0) STAMP(2, 1);
  const float gsr = lo16(pk2(gs, 0.f));
  const float gg = lo16(pk2(__builtin_amdgcn_rcpf(1.f + __builtin_amdgcn_exp2f(-gsr * 1.44269504088896f)), 0.f));
  const float g1 = lo16(pk2(1.f - gg, 0.f));
  lds_char* bb = sm + K3_BT + (4 * g + q) * 128 + 32 * w + 8 * p;
  u16* orow = out + (size_t)srow * D + cg * 72 + 4 * g;
#pragma unroll
  for (int t = 0; t < 5; ++t) {
    u32x2 bv = trread(t < 4 ? bb + t * 16 * 128 : sm + K3_BT + (64 + 4 * (g & 1) + q) * 128 + 32 * w + 8 * p);
    u32 a0 = pk2(acc[t][0], acc[t][1]), a1 = pk2(acc[t][2], acc[t][3]);
    u32 ga0 = pk2(gg * lo16(a0), gg * hi16(a0)), ga1 = pk2(gg * lo16(a1), gg * hi16(a1));
    u32 gb0 = pk2(g1 * lo16(bv.x), g1 * hi16(bv.x)), gb1 = pk2(g1 * lo16(bv.y), g1 * hi16(bv.y));
    u32x2 o = {pk2(lo16(ga0) + lo16(gb0), hi16(ga0) + hi16(gb0)), pk2(lo16(ga1) + lo16(gb1), hi16(ga1) + hi16(gb1))};
    if (t < 4 || g < 2) *(u32x2*)(orow + 16 * t) = o;
  }
  if (w == 0) STAMP(2, 2);
}

extern "C" void kernel_launch(void* const* d_in, const int* in_sizes, int n_in, void* d_out, int out_size, void* d_ws,
                              size_t ws_size, hipStream_t stream) {
  const u16* x = (const u16*)d_in[0];
  char* ws = (char*)d_ws;
  (void)hipFuncSetAttribute((const void*)k1, hipFuncAttributeMaxDynamicSharedMemorySize, K1_LDS);
  (void)hipFuncSetAttribute((const void*)k3, hipFuncAttributeMaxDynamicSharedMemorySize, K3_LDS);
  k1<<<256, 576, K1_LDS, stream>>>(x, (const u16*)d_in[1], (const u16*)d_in[4], (const u16*)d_in[5], (const u16*)d_in[6],
                                   (const u16*)d_in[7], (const u16*)d_in[8], (const u16*)d_in[9], (const u16*)d_in[10],
                                   (const u16*)d_in[12], ws);
  k2<<<272, 256, K2_LDS, stream>>>((const u16*)d_in[11], ws);
  k3<<<256, 512, K3_LDS, stream>>>((const u16*)d_in[2], ws, (u16*)d_out);
}
```

```cpp
#include <hip/hip_runtime.h>
#include <stdint.h>

typedef unsigned short u16;
typedef unsigned int u32;
typedef __attribute__((ext_vector_type(8))) __bf16 bf16x8;
typedef __attribute__((ext_vector_type(2))) __bf16 bf16x2;
typedef __attribute__((ext_vector_type(16))) float f32x16;
typedef __attribute__((ext_vector_type(4))) float f32x4;
typedef __attribute__((ext_vector_type(2))) float f32x2;
typedef __attribute__((ext_vector_type(6))) u32 u32x6;
typedef __attribute__((ext_vector_type(4))) u32 u32x4;
typedef __attribute__((ext_vector_type(2))) u32 u32x2;
typedef __attribute__((ext_vector_type(4))) short s16x4;
typedef __attribute__((address_space(3))) char lds_char;
typedef __attribute__((address_space(3))) void lds_void;
typedef __attribute__((address_space(3))) s16x4 lds_s16x4;
typedef __attribute__((address_space(3))) u32x4 lds_u32x4;
typedef __attribute__((address_space(3))) u32x2 lds_u32x2;
typedef __attribute__((address_space(3))) u32 lds_u32;
typedef __attribute__((address_space(3))) volatile u32 lds_vu32;
typedef __attribute__((address_space(3))) u16 lds_u16;
typedef __amdgpu_buffer_rsrc_t rsrc_t;

constexpr int S = 2048, D = 576;
constexpr u32 OFF_EP = 0;
constexpr u32 OFF_G = 512;
constexpr u32 OFF_KVT = OFF_G + 8192;
constexpr u32 OFF_WOP = OFF_KVT + 24576;
constexpr u32 OFF_PQ = OFF_WOP + 8 * 82944 + 4096;
constexpr u32 OFF_BT = OFF_PQ + 128 * 18432;
constexpr u32 OFF_KVP = OFF_BT + 576 * 4096;
constexpr u32 OFF_END = OFF_KVP + 32 * 16 * 2816;
constexpr int OOB = 0x40000000;

#define STAMP(k, s) do {} while (0)
#define TS(v) do {} while (0)
#define TSW(k, s, v) do {} while (0)

__device__ __forceinline__ u32 pk2(float a, float b) {
  f32x2 v = {a, b};
  bf16x2 r = __builtin_convertvector(v, bf16x2);
  return __builtin_bit_cast(u32, r);
}
__device__ __forceinline__ float lo16(u32 v) { return __uint_as_float(v << 16); }
__device__ __forceinline__ float hi16(u32 v) { return __uint_as_float(v & 0xffff0000u); }
__device__ __forceinline__ f32x4 mfma(u32x4 a, u32x4 b, f32x4 c) {
  return __builtin_amdgcn_mfma_f32_16x16x32_bf16(__builtin_bit_cast(bf16x8, a), __builtin_bit_cast(bf16x8, b), c, 0, 0, 0);
}
__device__ __forceinline__ f32x16 mfma32(u32x4 a, u32x4 b, f32x16 c) {
  return __builtin_amdgcn_mfma_f32_32x32x16_bf16(__builtin_bit_cast(bf16x8, a), __builtin_bit_cast(bf16x8, b), c, 0, 0, 0);
}
__device__ __forceinline__ u32x2 trread(lds_char* p) {
  s16x4 v = __builtin_amdgcn_ds_read_tr16_b64_v4i16((lds_s16x4*)p);
  return __builtin_bit_cast(u32x2, v);
}
__device__ __forceinline__ u32x4 cat(u32x2 a, u32x2 b) { return (u32x4){a.x, a.y, b.x, b.y}; }
__device__ __forceinline__ f32x2 cmul(f32x2 a, f32x2 b) {
  f32x2 t = {a.x * b.x, a.x * b.y};
  f32x2 s = {-b.y, b.x};
  f32x2 ay = {a.y, a.y};
  return __builtin_elementwise_fma(ay, s, t);
}
__device__ __forceinline__ rsrc_t mkrsrc(const void* p, u32 bytes) {
  return __builtin_amdgcn_make_buffer_rsrc((void*)p, 0, bytes, 0x00020000);
}
__device__ __forceinline__ void dma16(rsrc_t r, lds_char* dst, int voff, int soff) {
  __builtin_amdgcn_raw_ptr_buffer_load_lds(r, (lds_void*)dst, 16, voff, soff, 0, 0);
}
__device__ __forceinline__ u32 phi2(float a0, float a1) {
  constexpr float L2E = 1.44269504088896f;
  f32x2 a = {a0, a1};
  f32x2 ar = __builtin_elementwise_fma(a, (f32x2){L2E, L2E}, (f32x2){-L2E, -L2E});
  f32x2 m = {__builtin_amdgcn_fmed3f(a0, __builtin_amdgcn_exp2f(ar.x), 1.f), __builtin_amdgcn_fmed3f(a1, __builtin_amdgcn_exp2f(ar.y), 1.f)};
  m += (f32x2){1e-6f, 1e-6f};
  return pk2(m.x, m.y);
}
#define DG(t) do {} while (0)
#define DGC(v, t, idx) do {} while (0)
#define DGW(v, wv) do {} while (0)
#define LG0() do {} while (0)
#define BARRIER() asm volatile("s_barrier" ::: "memory")
#define WAITVM(n) asm volatile("s_waitcnt vmcnt(" #n ")" ::: "memory")
#define WAITLG0() do { __builtin_amdgcn_s_waitcnt(0xC07F); asm volatile("" ::: "memory"); } while (0)
#define SB() __builtin_amdgcn_sched_barrier(0)

constexpr int K1_LDS = 163840;

template <bool Q>
__device__ __forceinline__ void g1_load(lds_char* sm, int lw, int lane, const u16* x, const u16* wqkv, int rb, int cj, int rot, char* ws) {
  constexpr int SL = Q ? 40960 : 34816;
  constexpr int NB = Q ? 6 : 5;
  const rsrc_t rx = mkrsrc(x, S * D * 2), rw = mkrsrc(wqkv, D * 3 * D * 2);
  int va[4], vb[NB];
  {
    const int r8 = lane >> 3;
    const int ab = __umul24(rb * 128 + lw * 32 + r8, 1152);
    const int c0 = ((lane & 7) ^ (r8 >> 1)) << 4;
    va[0] = ab + c0; va[1] = ab + 8 * 1152 + (c0 ^ 64); va[2] = va[0] + 16 * 1152; va[3] = va[1] + 16 * 1152;
  }
  if (Q) {
#pragma unroll
    for (int j = 0; j < 3; ++j) {
      int idx = j * 64 + lane;
      int rho = __umul24(idx, 2731) >> 16, pos = idx - __umul24(rho, 24);
      int b0 = __umul24(lw * 16 + rho, 3456) + cj * 384, sw = (pos ^ (rho & 2)) << 4;
      vb[j] = b0 + sw;
      vb[j + 3] = b0 + 8 * 3456 + (sw ^ 64);
    }
  } else {
    const int u = ((lane >> 1) & 7) ^ (lane >> 4);
    const int b0 = __umul24(lw * 16 + (lane >> 4), 3456) + 1152 + cj * 144 + (u & 2) * 576 + ((lane & 1) << 4) + ((u & 1) << 5);
    vb[0] = b0 + ((u & 4) << 4); vb[1] = vb[0] + 4 * 3456;
    vb[2] = b0 + 8 * 3456 + ((~u & 4) << 4); vb[3] = vb[2] + 4 * 3456;
    const int rho = (lane >> 1) & 15;
    const int kr = (rho & 3) | ((rho & 4) << 1) | ((rho & 8) >> 1);
    vb[4] = __umul24(lw * 16 + kr, 3456) + 1152 + 128 + cj * 144 + (lane & 1) * 1152;
  }
  int st = rot;
  TS(t0);
  unsigned long long ta, tb, tc; u32 dv = 0;
#pragma unroll 1
  for (int it = 0; it < 9; ++it) {
    DG(ta);
    lds_char* base = sm + (it & 3) * SL;
#pragma unroll
    for (int j = 0; j < 4; ++j) dma16(rx, base + (lw * 4 + j) * 1024, va[j], st * 128);
#pragma unroll
    for (int j = 0; j < NB; ++j) {
      if (Q) dma16(rw, base + 16384 + (lw * 6 + j) * 1024, vb[j], st * 64 * 3456);
      else if (j < 4) dma16(rw, base + 16384 + (lw * 4 + j) * 1024, vb[j], st * 64 * 3456);
      else if (lane < 32) dma16(rw, base + 32768 + lw * 512, vb[j], st * 64 * 3456);
    }
    st = st == 8 ? 0 : st + 1;
    DG(tb);
    if (it < 2) { LG0(); DGC(dv, ta, 3 * it); DGC(dv, tb, 3 * it + 1); }
    if (it >= 2) {
      if (Q) WAITVM(20); else WAITVM(18);
      DG(tc); LG0(); DGC(dv, ta, 3 * it); DGC(dv, tb, 3 * it + 1); DGC(dv, tc, 3 * it + 2);
      if (lw == 0 && it == 2) STAMP(0, 6);
      BARRIER();
    }
  }
  if (Q) WAITVM(10); else WAITVM(9);
  BARRIER();
  WAITVM(0);
  DG(ta); LG0(); DGC(dv, ta, 27);
  if (lw == 0) { STAMP(0, 7); TSW(0, 5, t0); }
  BARRIER();
  DGW(dv, lw);
}

template <bool Q>
__device__ __forceinline__ void g1_main(lds_char* sm, int w, int lane, f32x4 (&acc)[4][Q ? 6 : 5], char* ws) {
  constexpr int SL = Q ? 40960 : 34816;
  constexpr int NT = Q ? 6 : 5, NS = 2 * NT, RING = Q ? 6 : 5, PD = 4;
  constexpr int RS = Q ? 384 : 256;
  const int wr = w >> 1, wc = w & 1;
  const int r = lane & 15, g = lane >> 4, q = (lane >> 2) & 3, p = lane & 3;
  const int sw = (r >> 1) & 7;
  lds_char* ao[2][2];
  lds_char* bo[5];
#pragma unroll
  for (int kk = 0; kk < 2; ++kk) {
    int a = (64 * wr + r) * 128 + (((4 * kk + g) ^ sw) << 4);
    ao[kk][0] = sm + (Q ? a : a + 4096 * wc);
    ao[kk][1] = sm + (Q ? a + 4096 : a + 4096 * (1 - wc));
  }
  if (Q) {
    const int f = (q >> 1) | ((g & 1) << 1);
#pragma unroll
    for (int t = 0; t < 4; ++t) {
      int T = 6 * wc + t;
      bo[t] = sm + (16384 + (8 * g + q) * 384 + 8 * p + (((T & 3) ^ f) << 5) + ((T >> 2) << 7));
    }
  } else {
    const int f = q | ((g & 1) << 2);
#pragma unroll
    for (int t = 0; t < 4; ++t) bo[t] = sm + (16384 + (8 * g + q) * 256 + 8 * p + (((4 * wc + t) ^ f) << 5));
    bo[4] = sm + (32768 + (16 * (g >> 1) + 4 * (g & 1) + q) * 32 + 8 * p);
  }
  u32x4 A[2][4];
  u32x4 B[RING];
  auto rdA = [&](int kk) {
    A[kk][0] = *(lds_u32x4*)(ao[kk][0]);
    A[kk][1] = *(lds_u32x4*)(ao[kk][0] + 2048);
    A[kk][2] = *(lds_u32x4*)(Q ? ao[kk][0] + 4096 : ao[kk][1]);
    A[kk][3] = *(lds_u32x4*)(Q ? ao[kk][0] + 6144 : ao[kk][1] + 2048);
  };
  auto rdB = [&](int u) {
    const int kk = u / NT, t = u % NT;
    if (!Q && t == 4) { B[u % RING] = cat(trread(bo[4] + kk * 1024), trread(bo[4] + kk * 1024 + 256)); return; }
    lds_char* a = bo[t & 3] + (t >> 2) * 128 + kk * 32 * RS;
    B[u % RING] = cat(trread(a), trread(a + 4 * RS));
  };
#pragma unroll
  for (int i = 0; i < 4; ++i)
#pragma unroll
    for (int t = 0; t < NT; ++t)
      if (Q || t < 4 || i < 2) asm volatile("" : "+v"(acc[i][t]));
  asm volatile("" : "+v"(ao[0][0]), "+v"(ao[1][0]), "+v"(bo[0]), "+v"(bo[1]), "+v"(bo[2]), "+v"(bo[3]));
  if (!Q) asm volatile("" : "+v"(ao[0][1]), "+v"(ao[1][1]), "+v"(bo[4]));
  unsigned long long ta, tb; u32 dv = 0;
  DG(ta); LG0(); DGC(dv, ta, 0);
  BARRIER();
  DG(tb);
  rdA(0);
#pragma unroll
  for (int u = 0; u < PD; ++u) rdB(u);
  SB();
#pragma unroll 1
  for (int st = 0; st < 9; ++st) {
#pragma unroll
    for (int u = 0; u < NS; ++u) {
      if (u == 1) rdA(1);
      if (u + PD < NS) rdB(u + PD);
      else if (u == NS - PD + 1) {
        DG(ta);
        WAITLG0();
        DGC(dv, tb, 1 + 2 * st); DGC(dv, ta, 2 + 2 * st);
        BARRIER();
        DG(tb);
        const int dl = (st & 3) == 3 ? -3 * SL : SL;
        ao[0][0] += dl; ao[1][0] += dl; bo[0] += dl; bo[1] += dl; bo[2] += dl; bo[3] += dl;
        if (!Q) { ao[0][1] += dl; ao[1][1] += dl; bo[4] += dl; }
        rdB(0); rdA(0); rdB(1);
      } else if (u > NS - PD + 1) rdB(u + PD - NS);
      const int kk = u / NT, t = u % NT;
#pragma unroll
      for (int i = 0; i < 4; ++i) {
        if (!Q && t == 4 && i >= 2) continue;
        acc[i][t] = Q ? mfma(B[u % RING], A[kk][i], acc[i][t]) : mfma(A[kk][i], B[u % RING], acc[i][t]);
      }
      SB();
    }
  }
  DG(ta); LG0(); DGC(dv, tb, 19); DGC(dv, ta, 20);
  DGW(dv, 4 + w);
}

__device__ __forceinline__ u32x4 band(int b, int g, int off, int len, int sh) {
  u32x4 r;
#pragma unroll
  for (int i = 0; i < 4; ++i) {
    int c0 = 8 - 8 * g + 2 * i - sh - (b - off - len + 1);
    r[i] = ((unsigned)c0 < (unsigned)len ? 0x3f80u : 0u) | ((unsigned)(c0 + 1) < (unsigned)len ? 0x3f800000u : 0u);
  }
  return r;
}

constexpr int TROW = 544;
constexpr int TBW = 13632;
constexpr int UOFF = 8 * TBW;
constexpr int UW = 4608;
constexpr int XG = 2320, XW = 9280;

__global__ __launch_bounds__(640, 1) void k1(const u16* __restrict__ x, const u16* __restrict__ wqkv, const u16* __restrict__ wo,
                                             const u16* __restrict__ Are, const u16* __restrict__ Aim,
                                             const u16* __restrict__ Bre, const u16* __restrict__ Bim,
                                             const u16* __restrict__ Cre, const u16* __restrict__ Cim,
                                             const u16* __restrict__ logdt, const u16* __restrict__ dskip,
                                             const u16* __restrict__ gw, char* __restrict__ ws) {
  extern __shared__ __attribute__((aligned(16))) char shm_[];
  lds_char* sm = (lds_char*)shm_;
  const int tid = threadIdx.x, lane = tid & 63;
  const int w = __builtin_amdgcn_readfirstlane(tid >> 6);
  const int xcd = blockIdx.x & 7, slot = blockIdx.x >> 3;
  const int c = lane & 15, g = lane >> 4;
  const f32x4 z4 = {0.f, 0.f, 0.f, 0.f};
  if (w == 0) STAMP(0, 0);
  if (__builtin_expect(slot < 16 && w < 4, 1)) {
    g1_load<false>(sm, w, lane, x, wqkv, (xcd >> 1) * 4 + (slot >> 2), (xcd & 1) * 4 + (slot & 3), (slot * 5) % 9, ws);
    return;
  }
  if (__builtin_expect(slot < 22 && w < 4, 1)) {
    g1_load<true>(sm, w, lane, x, wqkv, 2 * xcd + (slot - 16) / 3, (slot - 16) % 3, (slot * 5) % 9, ws);
    return;
  }
  if (slot < 16) {
    const int rb = (xcd >> 1) * 4 + (slot >> 2), kvj = (xcd & 1) * 4 + (slot & 3);
    if (w >= 8) return;
    const int cw = w - 4, wr = cw >> 1, wc = cw & 1;
    const u32x4 epw = *(const u32x4*)(ws + OFF_EP + (kvj * 4 + cw) * 16);
    f32x4 acc[4][5];
    const float i9 = c < 8 ? 1.f : 0.f;
#pragma unroll
    for (int i = 0; i < 4; ++i) {
      acc[i][0] = acc[i][1] = (f32x4){1.f, 1.f, 1.f, 1.f};
      acc[i][2] = acc[i][3] = z4;
      acc[i][4] = (f32x4){i9, i9, i9, i9};
    }
    g1_main<false>(sm, cw, lane, acc, ws);
    if (cw == 0) STAMP(0, 1);
    lds_char* X = sm + 34816;
    {
      lds_char* x0 = X + (2 * wr + wc) * XW + g * XG + c * 16;
      lds_char* x1 = X + (2 * wr + 1 - wc) * XW + g * XG + c * 16;
#pragma unroll
      for (int ip = 0; ip < 2; ++ip) {
        lds_char* xb = (ip ? x1 : x0) + wc * 512;
#pragma unroll
        for (int t = 2; t < 4; ++t) {
          f32x4 a = acc[2 * ip][t], b = acc[2 * ip + 1][t];
          *(lds_u32x4*)(xb + 1152 + (t - 2) * 256) = (u32x4){pk2(a[0], a[1]), pk2(a[2], a[3]), pk2(b[0], b[1]), pk2(b[2], b[3])};
        }
      }
      if (lane < 4) *(lds_u32x4*)(X + cw * XW + lane * XG + 2304) = (u32x4){0x3f803f80u, 0x3f803f80u, 0x3f803f80u, 0x3f803f80u};
#pragma unroll
      for (int ip = 0; ip < 2; ++ip) {
        lds_char* xb = (ip ? x1 : x0) + wc * 512;
#pragma unroll
        for (int t = 0; t < 2; ++t) {
          f32x4 a = acc[2 * ip][t], b = acc[2 * ip + 1][t];
          *(lds_u32x4*)(xb + t * 256) = (u32x4){phi2(a[0], a[1]), phi2(a[2], a[3]), phi2(b[0], b[1]), phi2(b[2], b[3])};
        }
      }
      {
        f32x4 a = acc[0][4], b = acc[1][4];
        u32x4 pk = {phi2(a[0], a[1]), phi2(a[2], a[3]), phi2(b[0], b[1]), phi2(b[2], b[3])};
        u32x4 pv = {pk2(a[0], a[1]), pk2(a[2], a[3]), pk2(b[0], b[1]), pk2(b[2], b[3])};
        *(lds_u32x4*)(x0 + (c < 8 ? 1024 : 2048)) = c < 8 ? pk : pv;
      }
    }
    WAITLG0();
    BARRIER();
    if (cw == 0) STAMP(0, 2);
    {
      const int e = lane & 31, hi = lane >> 5;
      lds_char* pa = X + hi * XG + (18 * cw - 2 * wc + e) * 16;
      lds_char* pb = X + hi * XG + (e < 18 ? 72 + 18 * cw + e : 144) * 16;
      u32x4 fa[8], fb[8];
#pragma unroll
      for (int m = 0; m < 8; ++m) {
        fa[m] = *(lds_u32x4*)(pa + (m >> 1) * XW + (m & 1) * 2 * XG);
        fb[m] = *(lds_u32x4*)(pb + (m >> 1) * XW + (m & 1) * 2 * XG);
      }
      f32x16 kv = {0.f, 0.f, 0.f, 0.f, 0.f, 0.f, 0.f, 0.f, 0.f, 0.f, 0.f, 0.f, 0.f, 0.f, 0.f, 0.f};
#pragma unroll
      for (int m = 0; m < 8; ++m) kv = mfma32(fa[m], fb[m], kv);
      const rsrc_t rk = mkrsrc(ws + OFF_KVP, 32 * 16 * 2816);
      const int so = ((kvj * 4 + cw) * 16 + rb) * 2816;
      const int vo = e < 19 ? (hi * 19 + e) * 16 : OOB;
      const bool sw0 = wc && !hi;
      const float v0 = sw0 ? kv[10] : kv[0], v1 = sw0 ? kv[11] : kv[1];
      auto tg = [&](int qq) { int sl = qq * 38 + hi * 19 + e; u32 ev = sl < 44 ? epw.x : sl < 88 ? epw.y : sl < 132 ? epw.z : epw.w; return ev * 0x9E3779B1u + 0x7F4A7C15u; };
      u32 tag = tg(0);
      __builtin_amdgcn_raw_buffer_store_b128((u32x4){__float_as_uint(v0), tag, __float_as_uint(v1), tag}, rk, vo, so, 16);
#pragma unroll
      for (int qq = 1; qq < 4; ++qq) {
        tag = tg(qq);
        __builtin_amdgcn_raw_buffer_store_b128((u32x4){__float_as_uint(kv[2 * qq]), tag, __float_as_uint(kv[2 * qq + 1]), tag}, rk, vo + qq * 608, so, 16);
      }
      tag = tg(4);
      __builtin_amdgcn_raw_buffer_store_b128((u32x4){__float_as_uint(kv[8]), tag, __float_as_uint(kv[9]), tag}, rk, hi ? OOB : vo + 2432, so, 16);
    }
    if (cw == 0) STAMP(0, 3);
    return;
  }
  if (slot < 22) {
    const int rb = 2 * xcd + (slot - 16) / 3, qj = (slot - 16) % 3;
    if (w >= 8) return;
    const int cw = w - 4, wr = cw >> 1, wc = cw & 1;
    f32x4 acc[4][6];
#pragma unroll
    for (int i = 0; i < 4; ++i)
#pragma unroll
      for (int t = 0; t < 6; ++t) acc[i][t] = (f32x4){1.f, 1.f, 1.f, 1.f};
    g1_main<true>(sm, cw, lane, acc, ws);
    if (cw == 0) STAMP(0, 8);
    char* dst = ws + OFF_PQ + (size_t)(rb * 8 + wr * 4) * 18432 + (48 * qj + 24 * wc) * 128 + lane * 8;
#pragma unroll
    for (int i = 0; i < 4; ++i)
#pragma unroll
      for (int t = 0; t < 6; ++t) {
        f32x4 a = acc[i][t];
        *(u32x2*)(dst + i * 18432 + t * 512) = (u32x2){phi2(a[0], a[1]), phi2(a[2], a[3])};
      }
    if (cw == 0) STAMP(0, 9);
    return;
  }
  if (slot < 31) {
    const int ci = slot - 22;
    const int chunk = ci < 8 ? 8 * xcd + ci : 64 + xcd;
    if (w < 2) {
      const u32 tag = *(const u32*)(ws + OFF_EP + (((xcd * 8 + ci) * 2 + w) & 127) * 4) * 0x9E3779B1u + 0x7F4A7C15u;
      const char* xs = (const char*)x + (size_t)(w * 1024 + 4 * lane) * 1152 + chunk * 16;
      u32x4 xv[4][4];
#pragma unroll
      for (int i = 0; i < 4; ++i)
#pragma unroll
        for (int j = 0; j < 4; ++j) xv[i][j] = *(const u32x4*)(xs + (i * 256 + j) * 1152);
      {
        lds_char* zp = sm + UOFF + (4 * w + (lane >> 4)) * UW + c * 32;
        if (c < 15) { *(lds_u32x4*)(zp) = (u32x4){0u, 0u, 0u, 0u}; *(lds_u32x4*)(zp + 16) = (u32x4){0u, 0u, 0u, 0u}; }
      }
      lds_char* ub = sm + UOFF + 480 + w * 2048 + lane * 8;
#pragma unroll
      for (int i = 0; i < 4; ++i)
#pragma unroll
        for (int k = 0; k < 4; ++k) {
          *(lds_u32x2*)(ub + i * 512 + (2 * k) * UW) = (u32x2){__builtin_amdgcn_perm(xv[i][1][k], xv[i][0][k], 0x05040100u), __builtin_amdgcn_perm(xv[i][3][k], xv[i][2][k], 0x05040100u)};
          *(lds_u32x2*)(ub + i * 512 + (2 * k + 1) * UW) = (u32x2){__builtin_amdgcn_perm(xv[i][1][k], xv[i][0][k], 0x07060302u), __builtin_amdgcn_perm(xv[i][3][k], xv[i][2][k], 0x07060302u)};
        }
      WAITLG0();
      if (w == 0) STAMP(0, 10);
      BARRIER();
      if (ci == 8) return;
      const int rid = (xcd * 8 + ci) * 2 + w, head = rid >> 2, qt = rid & 3;
      const int sl = 44 * qt + lane;
      const bool act = lane < 44 && sl < 171;
      const int qq = sl / 38, rem = sl - 38 * qq, hi = rem >= 19 ? 1 : 0;
      int e = rem - 19 * hi, plane, off;
      if (head & 1) {
        int R = 2 * (qq >> 1) + hi, jp = qq & 1;
        if (sl < 19) { R = 4; jp = 1; }
        plane = R < 1 ? 1 : R; off = R == 0 ? 4 : 8 + 4 * jp;
      } else {
        int G5 = 2 * (qq >> 1) + hi;
        plane = G5 == 0 ? 0 : G5 + 1; off = 4 * (qq & 1);
        if (qq == 4) { plane = 0; off = 8; }
      }
      if (lane >= 44) { e = lane - 44; plane = head & 1; off = (head & 1) ? 0 : 12; }
      int dsto = (e < 16 ? plane * 256 + e * 16 : 1280 + (e - 16) * 80 + plane * 16) + off;
      if (lane >= 44 && qt == 1) dsto = 1520 + (lane - 44) * 4;
      const bool wr_ = act || (qt == 0 && lane >= 44 && lane < 63) || (qt == 1 && lane >= 44 && lane < 48);
      const rsrc_t rk = mkrsrc(ws + OFF_KVP, 32 * 16 * 2816);
      const int so = head * 16 * 2816;
      {
        const int po = lane < 16 ? lane * 2816 + (qt == 3 ? 2432 : qt * 704 + 688) : OOB;
#pragma unroll 1
        for (int sp = 0; sp < 20000; ++sp) {
          asm volatile("" ::: "memory");
          u32x2 v = __builtin_amdgcn_raw_buffer_load_b64(rk, po, so, 16);
          if (__builtin_amdgcn_ballot_w64(v.y == tag && lane < 16) != 0) break;
          __builtin_amdgcn_s_sleep(2);
        }
      }
      if (w == 0) STAMP(0, 11);
      const int vo = act ? sl * 16 : OOB;
      float s0, s1;
#pragma unroll 1
      for (int sp = 0; sp < 20000; ++sp) {
        u32x4 v[16];
        asm volatile("" ::: "memory");
#pragma unroll
        for (int i = 0; i < 16; ++i) v[i] = __builtin_amdgcn_raw_buffer_load_b128(rk, vo, so + i * 2816, 16);
        u32 bad = 0; s0 = 0.f; s1 = 0.f;
#pragma unroll
        for (int i = 0; i < 16; ++i) {
          bad |= (v[i].y ^ tag) | (v[i].w ^ tag);
          s0 += __uint_as_float(v[i].x); s1 += __uint_as_float(v[i].z);
        }
        if (__builtin_amdgcn_ballot_w64(bad != 0 && act) == 0) break;
      }
      if (wr_) *(u32*)(ws + OFF_KVT + (head >> 1) * 1536 + dsto) = pk2(s0, s1);
      if (lane == 0) *(u32*)(ws + OFF_EP + rid * 4) = tag;
      if (w == 0) STAMP(0, 12);
      return;
    }
    const int cw = w - 2;
    const int d = chunk * 8 + cw;
    const u32 vAre = *(const u32*)(Are + d * 128 + 2 * lane), vAim = *(const u32*)(Aim + d * 128 + 2 * lane);
    const u32 vBre = *(const u32*)(Bre + d * 128 + 2 * lane), vBim = *(const u32*)(Bim + d * 128 + 2 * lane);
    const u32 vCre = *(const u32*)(Cre + d * 128 + 2 * lane), vCim = *(const u32*)(Cim + d * 128 + 2 * lane);
    const u16 vld = logdt[d];
    const float dsk = lo16(dskip[d]);
    lds_char* tV = sm + cw * TBW;
    lds_char* si = tV + 16 * TROW;
    u32x4 bd[6];
    bd[0] = band(c, g, 3, 3, 0); bd[1] = band(c, g, 6, 5, 0); bd[2] = band(c, g, 11, 3, 0); bd[3] = band(c, g, 14, 5, 8);
    bd[4] = band(c, g, 5, 7, 0); bd[5] = band(c, g, 12, 9, 8);
    u32x4 fixm;
#pragma unroll
    for (int i = 0; i < 4; ++i) {
      int l0 = c + 8 * g - 2 * i - 11;
      fixm[i] = ((unsigned)l0 < 2u ? 0xffffu : 0u) | ((unsigned)(l0 - 1) < 2u ? 0xffff0000u : 0u);
    }
    {
      const float dt = lo16(pk2(__builtin_amdgcn_exp2f(lo16(vld) * 1.44269504088896f), 0.f));
      f32x2 w1[2], w16[2], w256[2], cb[2];
#pragma unroll
      for (int m = 0; m < 2; ++m) {
        float ar = dt * (m ? hi16(vAre) : lo16(vAre)), ai = dt * (m ? hi16(vAim) : lo16(vAim));
        float br = dt * (m ? hi16(vBre) : lo16(vBre)), bi = dt * (m ? hi16(vBim) : lo16(vBim));
        float cr = m ? hi16(vCre) : lo16(vCre), cim = m ? hi16(vCim) : lo16(vCim);
        cb[m] = (f32x2){cr * br - cim * bi, -(cr * bi + cim * br)};
        float le = ar * 1.44269504088896f, rv = ai * 0.15915494309189535f;
        float e1 = __builtin_amdgcn_exp2f(le), e16 = __builtin_amdgcn_exp2f(le * 16.f), e256 = __builtin_amdgcn_exp2f(le * 256.f);
        float r1 = __builtin_amdgcn_fractf(rv), r16 = __builtin_amdgcn_fractf(rv * 16.f), r256 = __builtin_amdgcn_fractf(rv * 256.f);
        w1[m] = (f32x2){e1 * __builtin_amdgcn_cosf(r1), -e1 * __builtin_amdgcn_sinf(r1)};
        w16[m] = (f32x2){e16 * __builtin_amdgcn_cosf(r16), e16 * __builtin_amdgcn_sinf(r16)};
        w256[m] = (f32x2){e256 * __builtin_amdgcn_cosf(r256), -e256 * __builtin_amdgcn_sinf(r256)};
      }
      auto put = [&](lds_char* t, int row, f32x2 a, f32x2 b) {
        *(lds_u32x2*)(t + row * TROW + lane * 8) = (u32x2){pk2(a.x, a.y), pk2(b.x, b.y)};
      };
      f32x2 ko[4][2];
      {
        f32x2 v0 = w16[0], v1 = w16[1];
        f32x2 w2[2] = {cmul(w1[0], w1[0]), cmul(w1[1], w1[1])};
        f32x2 u0 = cmul(cb[0], w256[0]), u1 = cmul(cb[1], w256[1]);
        *(lds_u32x2*)(tV + lane * 8) = (u32x2){0x00003f80u, 0x00003f80u};
        put(tV, 1, v0, v1);
        put(si, 8, u0, u1);
#pragma unroll
        for (int e = 2; e < 16; ++e) {
          v0 = cmul(v0, w16[0]); v1 = cmul(v1, w16[1]); put(tV, e, v0, v1);
          if (e < 10) {
            u0 = cmul(u0, w2[0]); u1 = cmul(u1, w2[1]);
            put(si, e - 2, u0, u1);
            if (e & 1) { ko[(e - 2) >> 1][0] = u0; ko[(e - 2) >> 1][1] = u1; }
          }
        }
      }
      const int fr = c * TROW + g * 16;
      u32x4 Vf[8];
      f32x4 t0 = z4, ta = z4, tbb = z4;
#pragma unroll
      for (int kk = 0; kk < 8; ++kk) {
        Vf[kk] = *(lds_u32x4*)(tV + fr + kk * 64);
        u32x4 af = *(lds_u32x4*)(si + fr + kk * 64);
        ta = mfma(af, Vf[kk], ta);
      }
      {
        f32x2 u0 = cb[0], u1 = cb[1];
        put(tV, 0, u0, u1);
#pragma unroll
        for (int e = 1; e < 16; ++e) { u0 = cmul(u0, w1[0]); u1 = cmul(u1, w1[1]); put(tV, e, u0, u1); }
      }
#pragma unroll
      for (int kk = 0; kk < 8; ++kk) t0 = mfma(*(lds_u32x4*)(tV + fr + kk * 64), Vf[kk], t0);
      const u32x4 zz = {0u, 0u, 0u, 0u};
      *(lds_u32x4*)(si + lane * 16) = zz;
      *(lds_u32x4*)(si + 1024 + lane * 16) = zz;
      if (lane < 4) *(lds_u32x4*)(si + 2048 + lane * 16) = zz;
#pragma unroll
      for (int o = 0; o < 4; ++o) {
        f32x2 a0 = cmul(ko[o][0], w256[0]), a1 = cmul(ko[o][1], w256[1]);
        put(tV, o, a0, a1);
        a0 = cmul(a0, w256[0]); a1 = cmul(a1, w256[1]);
        put(tV, 4 + o, a0, a1);
        if (o & 1) {
#pragma unroll
          for (int qq = 0; qq < 4; ++qq) {
            a0 = cmul(a0, w256[0]); a1 = cmul(a1, w256[1]);
            put(tV, 8 + 2 * qq + (o >> 1), a0, a1);
          }
        }
      }
#pragma unroll
      for (int kk = 0; kk < 8; ++kk) tbb = mfma(Vf[kk], *(lds_u32x4*)(tV + fr + kk * 64), tbb);
      *(lds_u32x2*)(si + 2 * (540 - 16 * c - 4 * g)) = (u32x2){pk2(t0[3], t0[2]), pk2(t0[1], t0[0])};
      if (g < 2) {
        lds_u32* pR = (lds_u32*)(si + 2 * (278 - 16 * c - 8 * g));
        pR[0] = pk2(ta[3], ta[3]); pR[1] = pk2(ta[3], ta[2]); pR[2] = pk2(ta[1], ta[1]); pR[3] = pk2(ta[1], ta[0]);
        if (lane == 31) *(lds_u16*)(si + 2 * 29) = (u16)pk2(ta[3], ta[3]);
      } else if (lane == 32) {
        *(lds_u32*)(si + 2 * 286) = pk2(ta[0], ta[0]);
      }
      {
        int p0 = c < 8 ? 560 + 64 * (c & 3) + 44 - 16 * (c >> 2) - 4 * g : 816 + 80 * (c & 1) + 60 - 16 * ((c - 8) >> 1) - 4 * g;
        *(lds_u32x2*)(si + 2 * p0) = (u32x2){pk2(tbb[3], tbb[2]), pk2(tbb[1], tbb[0])};
      }
#pragma unroll
      for (int i = 0; i < 2; ++i) {
        u32x4 a = *(lds_u32x4*)(si + i * 1024 + lane * 16);
        u32 n = *(lds_u32*)(si + i * 1024 + lane * 16 + 16);
        *(lds_u32x4*)(si + 2112 + i * 1024 + lane * 16) = (u32x4){__builtin_amdgcn_alignbit(a.y, a.x, 16), __builtin_amdgcn_alignbit(a.z, a.y, 16), __builtin_amdgcn_alignbit(a.w, a.z, 16), __builtin_amdgcn_alignbit(n, a.w, 16)};
      }
    }
    if (cw == 0) STAMP(0, 13);
    if (cw == 7) STAMP(0, 14);
    BARRIER();
    lds_char* su = sm + UOFF + cw * UW;
    lds_char* ub = su + 480 + 32 * c + 16 - 16 * g;
    lds_char* kb = si + ((c & 1) ? 2 * (551 - 8 * g - c) : 2112 + 2 * (550 - 8 * g - c));
    lds_char* sb = si + ((c & 1) ? 2 * (79 - c + 4 * g) : 2112 + 2 * (78 - c + 4 * g));
    auto ufrag = [&](int s) { return *(lds_u32x4*)(ub + 64 * s); };
    auto kfrag = [&](int i) { lds_u32* pp = (lds_u32*)(kb - 64 * i); return (u32x4){pp[0], pp[1], pp[2], pp[3]}; };
    auto sfrag = [&](int e0, int e1) { lds_u32* pp = (lds_u32*)(sb + 2 * e0); lds_u32* q2 = (lds_u32*)(sb + 2 * e1); return (u32x4){pp[0], pp[1], q2[0], q2[1]}; };
    f32x4 acc[8];
    {
      u32x4 U[8], F2[6][2], F3[4];
#pragma unroll
      for (int m = 0; m < 8; ++m) U[m] = ufrag(8 * m);
      u32x4 A0 = kfrag(0), A1 = kfrag(8), A2 = kfrag(16);
      u32 k1024 = *(lds_u16*)(si + 2 * (560 + 192 + 16));
      k1024 |= k1024 << 16;
#pragma unroll
      for (int T = 0; T < 8; ++T) acc[T] = mfma(A0, U[T], z4);
#pragma unroll
      for (int m = 0; m < 6; ++m) {
        u32x4 Us = *(lds_u32x4*)(ub + 64 * 8 * m - 16);
        f32x4 f0 = mfma(U[m], bd[0], z4), f1 = mfma(U[m], bd[1], z4), f2 = mfma(U[m], bd[2], z4), f3 = mfma(Us, bd[3], z4);
        F2[m][0] = (u32x4){pk2(f0[0], f0[1]), pk2(f0[2], f0[3]), pk2(f1[0], f1[1]), pk2(f1[2], f1[3])};
        F2[m][1] = (u32x4){pk2(f2[0], f2[1]), pk2(f2[2], f2[3]), pk2(f3[0], f3[1]), pk2(f3[2], f3[3])};
        if (m < 4) {
          f32x4 f4 = mfma(U[m], bd[4], z4), f5 = mfma(Us, bd[5], z4);
          F3[m] = (u32x4){pk2(f4[0], f4[1]), pk2(f4[2], f4[3]), pk2(f5[0], f5[1]), pk2(f5[2], f5[3])};
        }
      }
#pragma unroll
      for (int T = 1; T < 8; ++T) acc[T] = mfma(A1, U[T - 1], acc[T]);
#pragma unroll
      for (int T = 2; T < 8; ++T) acc[T] = mfma(A2, U[T - 2], acc[T]);
      u32x4 fx = {fixm[0] & k1024, fixm[1] & k1024, fixm[2] & k1024, fixm[3] & k1024};
#pragma unroll
      for (int T = 4; T < 8; ++T) acc[T] = mfma(fx, U[T - 4], acc[T]);
#pragma unroll
      for (int dl = 2; dl <= 4; ++dl)
#pragma unroll
        for (int pr = 0; pr < 2; ++pr) {
          u32x4 Sf = sfrag(560 + 128 * pr - 16 * dl, 560 + 128 * pr + 64 - 16 * dl);
#pragma unroll
          for (int T = dl; T < 8; ++T) acc[T] = mfma(F2[T - dl][pr], Sf, acc[T]);
        }
#pragma unroll
      for (int dl = 4; dl < 8; ++dl) {
        u32x4 Sf = sfrag(816 + 48 - 16 * dl, 816 + 80 + 48 - 16 * dl);
#pragma unroll
        for (int T = dl; T < 8; ++T) acc[T] = mfma(F3[T - dl], Sf, acc[T]);
      }
    }
#pragma unroll
    for (int r = 1; r < 8; ++r) {
      u32x4 A0 = kfrag(r), A1 = kfrag(8 + r);
      u32x4 Up;
#pragma unroll
      for (int T = 0; T < 8; ++T) {
        u32x4 U = ufrag(8 * T - r);
        if (T > 0) acc[T] = mfma(A1, Up, acc[T]);
        acc[T] = mfma(A0, U, acc[T]);
        Up = U;
      }
    }
    if (cw == 0) STAMP(0, 15);
    char* gb = ws + OFF_BT + (size_t)d * 4096 + c * 32 + g * 8;
#pragma unroll
    for (int T = 0; T < 8; ++T) {
      u32x2 uu = *(lds_u32x2*)(su + 480 + 512 * T + 32 * c + 8 * g);
      u32x2 o = {pk2(fmaf(lo16(uu.x), dsk, acc[T][0]), fmaf(hi16(uu.x), dsk, acc[T][1])), pk2(fmaf(lo16(uu.y), dsk, acc[T][2]), fmaf(hi16(uu.y), dsk, acc[T][3]))};
      *(u32x2*)(gb + 512 * T) = o;
    }
    if (cw == 0) STAMP(0, 16);
    if (cw == 7) STAMP(0, 17);
    return;
  }
  if (w < 2) {
    const rsrc_t rw = mkrsrc(wo, D * D * 2);
#pragma unroll 1
    for (int ins = w; ins < 81; ins += 2) {
      int idx = ins * 64 + lane;
      int r = idx / 9, ch = idx - r * 9;
      dma16(rw, sm + ins * 1024, r * 1152 + xcd * 144 + ch * 16, 0);
    }
    WAITVM(0);
  } else {
    const int row = 256 * xcd + 32 * (w - 2) + c;
    const char* xs = (const char*)x + (size_t)row * 1152 + g * 16;
    u32x4 gf[18], xf[18];
#pragma unroll
    for (int k = 0; k < 18; ++k) { xf[k] = *(const u32x4*)(xs + k * 64); gf[k] = *(const u32x4*)((const char*)gw + k * 64 + g * 16); }
    SB();
    f32x4 a0 = z4, a1 = z4;
#pragma unroll
    for (int k = 0; k < 18; ++k) { a0 = mfma(gf[k], xf[k], a0); asm volatile("" : "+v"(xs), "+v"(a0)); xf[k] = *(const u32x4*)(xs + 16 * 1152 + k * 64); SB(); }
#pragma unroll
    for (int k = 0; k < 18; ++k) a1 = mfma(gf[k], xf[k], a1);
    const float gs = lo16(pk2(g == 0 ? a0[0] : a1[0], 0.f));
    const float gg = lo16(pk2(__builtin_amdgcn_rcpf(1.f + __builtin_amdgcn_exp2f(-gs * 1.44269504088896f)), 0.f));
    if (g < 2) *(u32*)(ws + OFF_G + (row + 16 * g) * 4) = pk2(gg, 1.f - gg);
  }
  if (w == 2) STAMP(0, 18);
  BARRIER();
  {
    const int q = (lane >> 2) & 3, p = lane & 3;
    char* dst = ws + OFF_WOP + xcd * 82944;
#pragma unroll 1
    for (int j = 0; j < 9; ++j) {
      const int f = w * 9 + j, si = f / 5, t = f - si * 5;
      const int colb = t < 4 ? 64 * (t >> 1) + 16 * p + 8 * (t & 1) : 128 + 8 * (p & 1);
      int r0, r1;
      if (si == 8 || si == 17) {
        const int m = si == 17;
        r0 = 36 * (8 * m + (q & 2) + (g >> 1)) + 18 * (g & 1) + 16 + (q & 1);
        r1 = r0 + 144;
      } else {
        const int pr = si < 8 ? si : si - 1;
        r0 = 36 * pr + 4 * g + q; r1 = r0 + 18;
      }
      u32x4 v = cat(trread(sm + r0 * 144 + colb), trread(sm + r1 * 144 + colb));
      if (t < 4) *(u32x4*)(dst + si * 4608 + t * 1024 + lane * 16) = v;
      else if (c < 8) *(u32x4*)(dst + si * 4608 + 4096 + g * 128 + c * 16) = v;
    }
  }
  if (w == 2) STAMP(0, 19);
}

constexpr int L_W = 24576, L_BT = L_W + 86016, K2_LDS = L_BT + 12288;

__global__ __launch_bounds__(512, 1) void k2(char* __restrict__ ws, u16* __restrict__ out) {
  extern __shared__ __attribute__((aligned(16))) char shm_[];
  lds_char* sm = (lds_char*)shm_;
  const int tid = threadIdx.x, lane = tid & 63;
  const int w = __builtin_amdgcn_readfirstlane(tid >> 6);
  const int xcd = blockIdx.x & 7, slot = blockIdx.x >> 3;
  const int row0 = 256 * xcd + 64 * (slot >> 3), cg = slot & 7;
  const rsrc_t rs = mkrsrc(ws, OFF_END);
  if (w == 0) STAMP(1, 0);
  if (w < 4) {
    const int vl = lane * 16;
    const int sk = OFF_KVT + w * 1024, sw = OFF_WOP + cg * 82944 + w * 1024;
    lds_char* lk = sm + w * 1024;
    lds_char* lwp = sm + L_W + w * 1024;
#pragma unroll
    for (int i = 0; i < 7; ++i) {
      if (i < 6) dma16(rs, lk + i * 4096, vl, sk + i * 4096);
#pragma unroll
      for (int j = 0; j < 3; ++j) dma16(rs, lwp + (3 * i + j) * 4096, vl, sw + (3 * i + j) * 4096);
    }
#pragma unroll
    for (int j = 0; j < 3; ++j) {
      int dd = (w + 4 * j) * 8 + (lane >> 3);
      int f = ((dd >> 1) & 1) | ((dd >> 2) & 2);
      dd = dd < 72 ? dd : 71;
      dma16(rs, sm + L_BT + (w + 4 * j) * 1024, dd * 4096 + (((lane & 7) ^ (f << 1)) << 4), OFF_BT + cg * 72 * 4096 + row0 * 2);
    }
    WAITVM(26); if (w == 1) STAMP(1, 4); BARRIER();
    WAITVM(22); BARRIER();
    WAITVM(18); BARRIER();
    WAITVM(14); BARRIER();
    WAITVM(10); BARRIER();
    WAITVM(6); BARRIER();
    WAITVM(3); BARRIER();
    WAITVM(0); if (w == 1) STAMP(1, 5); BARRIER();
    return;
  }
  const int cw = w - 4;
  const int c = lane & 15, g = lane >> 4, q = (lane >> 2) & 3, p4 = lane & 3;
  const int sq = OFF_PQ + (row0 / 16 + cw) * 18432;
  const int vq = lane * 8, vm = g == 0 ? lane * 8 + 512 : OOB;
  u32x6 P[16];
#pragma unroll
  for (int p = 0; p < 16; ++p) {
    u32x2 e = __builtin_amdgcn_raw_buffer_load_b64(rs, vq, sq + p * 1152, 0);
    u32x2 m = __builtin_amdgcn_raw_buffer_load_b64(rs, vm, sq + p * 1152, 0);
    u32x2 o = __builtin_amdgcn_raw_buffer_load_b64(rs, vq + 640, sq + p * 1152, 0);
    P[p] = (u32x6){e.x, e.y, m.x, m.y, o.x, o.y};
  }
  const int srow = row0 + 16 * cw + c;
  const u32 gv = *(const u32*)(ws + OFF_G + srow * 4);
  const int pe = g == 0 ? 0 : g + 1, cl = c & 3, cq = c >> 2;
  lds_char* aE = sm + pe * 256 + c * 16;
  lds_char* aO = sm + (g + 1) * 256 + c * 16;
  lds_char* aXE[2];
  lds_char* aXO[2];
#pragma unroll
  for (int b = 0; b < 2; ++b) {
    aXE[b] = sm + (cl == 0 ? 1440 + pe * 16 : (cl >= 2 && cq == 2 * b ? 1280 + (cl - 2) * 80 + pe * 16 : 1520));
    aXO[b] = sm + (cl == 1 ? 1440 + (g + 1) * 16 : (cl >= 2 && cq == 2 * b + 1 ? 1280 + (cl - 2) * 80 + (g + 1) * 16 : 1520));
  }
  lds_char* aW = sm + L_W + lane * 16;
  lds_char* aW4 = sm + L_W + 4096 + g * 128 + (c & 7) * 16;
  const f32x4 z4 = {0.f, 0.f, 0.f, 0.f}, eps4 = {1e-6f, 1e-6f, 0.f, 0.f};
  f32x4 acc[5];
#pragma unroll
  for (int t = 0; t < 5; ++t) acc[t] = z4;
  u32x4 KF[2][4], WF[2][5], XF[5], Ob, Xb;
  auto rdK = [&](int p) {
    KF[p & 1][0] = *(lds_u32x4*)(aXE[p & 1] + p * 1536);
    KF[p & 1][1] = *(lds_u32x4*)(aXO[p & 1] + p * 1536);
    KF[p & 1][2] = *(lds_u32x4*)(aE + p * 1536);
    KF[p & 1][3] = *(lds_u32x4*)(aO + p * 1536);
  };
  auto rdW = [&](u32x4 (&F)[5], int si) {
#pragma unroll
    for (int t = 0; t < 4; ++t) F[t] = *(lds_u32x4*)(aW + si * 4608 + t * 1024);
    F[4] = *(lds_u32x4*)(aW4 + si * 4608);
  };
  SB();
  BARRIER();
  if (cw == 0) STAMP(1, 1);
  rdK(0);
#pragma unroll
  for (int p = 0; p <= 16; ++p) {
    if (p == 1 || p == 4 || p == 7 || p == 9 || p == 12 || p == 15) BARRIER();
    if (p < 15) rdK(p + 1);
    if (p < 16) rdW(WF[p & 1], p < 8 ? p : p + 1);
    if (p == 8) rdW(XF, 8);
    if (p == 15) rdW(XF, 17);
    f32x4 X, E, O;
    if (p < 16) {
      u32x4 B1 = {P[p][0], P[p][1], P[p][2], P[p][3]}, B2 = {P[p][2], P[p][3], P[p][4], P[p][5]};
      X = mfma(KF[p & 1][0], B1, eps4);
      E = mfma(KF[p & 1][2], B1, z4);
      X = mfma(KF[p & 1][1], B2, X);
      O = mfma(KF[p & 1][3], B2, z4);
    }
    if (p > 0) {
#pragma unroll
      for (int t = 0; t < 5; ++t) acc[t] = mfma(WF[(p - 1) & 1][t], Ob, acc[t]);
    }
    if (p == 9 || p == 16) {
#pragma unroll
      for (int t = 0; t < 5; ++t) acc[t] = mfma(XF[t], Xb, acc[t]);
    }
    if (p < 16) {
      const float re = __builtin_amdgcn_rcpf(X[0]), ro = __builtin_amdgcn_rcpf(X[1]);
      const float rx = (g & 1) ? ro : re;
      E *= re; O *= ro;
      Ob = (u32x4){pk2(E[0], E[1]), pk2(E[2], E[3]), pk2(O[0], O[1]), pk2(O[2], O[3])};
      u32 xv = pk2(X[2] * rx, X[3] * rx);
      if (p == 8) {   }
      if (p & 1) Xb[(p >> 1) & 3] |= xv; else if (p != 8) Xb[(p >> 1) & 3] = xv;
      if (p == 8) P[8][0] = xv;
      if (p == 9) Xb[0] = P[8][0] | xv;
    }
    SB();
  }
  if (cw == 0) STAMP(1, 2);
  BARRIER();
  const float gg = lo16(gv), g1 = hi16(gv);
  const int f = (q >> 1) | ((g & 1) << 1);
  lds_char* bb = sm + L_BT + (8 * g + q) * 128 + ((cw ^ f) << 5) + 8 * p4;
  lds_char* bb4 = sm + L_BT + (64 + 4 * (g & 1) + q) * 128 + ((cw ^ (q >> 1)) << 5) + 8 * p4;
  u16* orow = out + (size_t)srow * D + cg * 72;
  auto mix = [&](f32x4 a, u32x2 b) {
    return (u32x2){pk2(fmaf(gg, a[0], g1 * lo16(b.x)), fmaf(gg, a[1], g1 * hi16(b.x))), pk2(fmaf(gg, a[2], g1 * lo16(b.y)), fmaf(gg, a[3], g1 * hi16(b.y)))};
  };
  u32x2 bv[5];
#pragma unroll
  for (int t = 0; t < 4; ++t) bv[t] = trread(bb + (t >> 1) * 4096 + (t & 1) * 512);
  bv[4] = trread(bb4);
#pragma unroll
  for (int u = 0; u < 2; ++u) *(u32x4*)(orow + 32 * u + 8 * g) = cat(mix(acc[2 * u], bv[2 * u]), mix(acc[2 * u + 1], bv[2 * u + 1]));
  u32x2 o4 = mix(acc[4], bv[4]);
  if (g < 2) *(u32x2*)(orow + 64 + 4 * g) = o4;
  if (cw == 0) STAMP(1, 3);
}

extern "C" void kernel_launch(void* const* d_in, const int* in_sizes, int n_in, void* d_out, int out_size, void* d_ws,
                              size_t ws_size, hipStream_t stream) {
  char* ws = (char*)d_ws;
  (void)hipFuncSetAttribute((const void*)k1, hipFuncAttributeMaxDynamicSharedMemorySize, K1_LDS);
  (void)hipFuncSetAttribute((const void*)k2, hipFuncAttributeMaxDynamicSharedMemorySize, K2_LDS);
  k1<<<256, 640, K1_LDS, stream>>>((const u16*)d_in[0], (const u16*)d_in[1], (const u16*)d_in[2], (const u16*)d_in[4], (const u16*)d_in[5],
                                   (const u16*)d_in[6], (const u16*)d_in[7], (const u16*)d_in[8], (const u16*)d_in[9],
                                   (const u16*)d_in[10], (const u16*)d_in[11], (const u16*)d_in[12], ws);
  k2<<<256, 512, K2_LDS, stream>>>(ws, (u16*)d_out);
}
```

```cpp
#include <hip/hip_runtime.h>
#include <stdint.h>

typedef unsigned short u16;
typedef unsigned int u32;
typedef __attribute__((ext_vector_type(8))) __bf16 bf16x8;
typedef __attribute__((ext_vector_type(2))) __bf16 bf16x2;
typedef __attribute__((ext_vector_type(16))) float f32x16;
typedef __attribute__((ext_vector_type(4))) float f32x4;
typedef __attribute__((ext_vector_type(2))) float f32x2;
typedef __attribute__((ext_vector_type(6))) u32 u32x6;
typedef __attribute__((ext_vector_type(4))) u32 u32x4;
typedef __attribute__((ext_vector_type(2))) u32 u32x2;
typedef __attribute__((ext_vector_type(4))) short s16x4;
typedef __attribute__((address_space(3))) char lds_char;
typedef __attribute__((address_space(3))) void lds_void;
typedef __attribute__((address_space(3))) s16x4 lds_s16x4;
typedef __attribute__((address_space(3))) u32x4 lds_u32x4;
typedef __attribute__((address_space(3))) u32x2 lds_u32x2;
typedef __attribute__((address_space(3))) u32 lds_u32;
typedef __attribute__((address_space(3))) volatile u32 lds_vu32;
typedef __attribute__((address_space(3))) u16 lds_u16;
typedef __amdgpu_buffer_rsrc_t rsrc_t;

constexpr int S = 2048, D = 576;
constexpr u32 OFF_EP = 0;
constexpr u32 OFF_G = 1024;
constexpr u32 OFF_KVT = OFF_G + 8192;
constexpr u32 OFF_PQ = OFF_KVT + 32768;
constexpr u32 OFF_BT = OFF_PQ + 128 * 18432;
constexpr u32 OFF_KVP = OFF_BT + 576 * 4096;
constexpr u32 OFF_END = OFF_KVP + 32 * 16 * 3072;
constexpr int OOB = 0x40000000;

#define STAMP(k, s) do {} while (0)
#define TS(v) do {} while (0)
#define TSW(k, s, v) do {} while (0)

__device__ __forceinline__ u32 pk2(float a, float b) {
  f32x2 v = {a, b};
  bf16x2 r = __builtin_convertvector(v, bf16x2);
  return __builtin_bit_cast(u32, r);
}
__device__ __forceinline__ float lo16(u32 v) { return __uint_as_float(v << 16); }
__device__ __forceinline__ float hi16(u32 v) { return __uint_as_float(v & 0xffff0000u); }
__device__ __forceinline__ f32x4 mfma(u32x4 a, u32x4 b, f32x4 c) {
  return __builtin_amdgcn_mfma_f32_16x16x32_bf16(__builtin_bit_cast(bf16x8, a), __builtin_bit_cast(bf16x8, b), c, 0, 0, 0);
}
__device__ __forceinline__ f32x16 mfma32(u32x4 a, u32x4 b, f32x16 c) {
  return __builtin_amdgcn_mfma_f32_32x32x16_bf16(__builtin_bit_cast(bf16x8, a), __builtin_bit_cast(bf16x8, b), c, 0, 0, 0);
}
__device__ __forceinline__ f32x4 mfma16(u32x2 a, u32x2 b, f32x4 c) {
  return __builtin_amdgcn_mfma_f32_16x16x16bf16_1k(__builtin_bit_cast(s16x4, a), __builtin_bit_cast(s16x4, b), c, 0, 0, 0);
}
__device__ __forceinline__ u32x2 trread(lds_char* p) {
  s16x4 v = __builtin_amdgcn_ds_read_tr16_b64_v4i16((lds_s16x4*)p);
  return __builtin_bit_cast(u32x2, v);
}
__device__ __forceinline__ u32x4 cat(u32x2 a, u32x2 b) { return (u32x4){a.x, a.y, b.x, b.y}; }
__device__ __forceinline__ f32x2 cmul(f32x2 a, f32x2 b) {
  f32x2 t = {a.x * b.x, a.x * b.y};
  f32x2 s = {-b.y, b.x};
  f32x2 ay = {a.y, a.y};
  return __builtin_elementwise_fma(ay, s, t);
}
__device__ __forceinline__ rsrc_t mkrsrc(const void* p, u32 bytes) {
  return __builtin_amdgcn_make_buffer_rsrc((void*)p, 0, bytes, 0x00020000);
}
__device__ __forceinline__ void dma16(rsrc_t r, lds_char* dst, int voff, int soff) {
  __builtin_amdgcn_raw_ptr_buffer_load_lds(r, (lds_void*)dst, 16, voff, soff, 0, 0);
}
__device__ __forceinline__ void st16(u32x4 v, rsrc_t r, int voff, int soff) {
  __builtin_amdgcn_raw_buffer_store_b128(v, r, voff, soff, 16);
  asm volatile("s_nop 1" :: "v"(v));
}
__device__ __forceinline__ u32x4 swap16(u32x2 a, u32x2 b) {
  auto r0 = __builtin_amdgcn_permlane16_swap(a.x, b.x, false, false);
  auto r1 = __builtin_amdgcn_permlane16_swap(a.y, b.y, false, false);
  return (u32x4){r0[0], r1[0], r0[1], r1[1]};
}
__device__ __forceinline__ u32 phi2(float a0, float a1) {
  constexpr float L2E = 1.44269504088896f;
  f32x2 a = {a0, a1};
  f32x2 ar = __builtin_elementwise_fma(a, (f32x2){L2E, L2E}, (f32x2){-L2E, -L2E});
  f32x2 m = {__builtin_amdgcn_fmed3f(a0, __builtin_amdgcn_exp2f(ar.x), 1.f), __builtin_amdgcn_fmed3f(a1, __builtin_amdgcn_exp2f(ar.y), 1.f)};
  m += (f32x2){1e-6f, 1e-6f};
  return pk2(m.x, m.y);
}
#define DG(t) do {} while (0)
#define DGC(v, t, idx) do {} while (0)
#define DGW(v, wv) do {} while (0)
#define LG0() do {} while (0)
#define BARRIER() asm volatile("s_barrier" ::: "memory")
#define WAITVM(n) asm volatile("s_waitcnt vmcnt(" #n ")" ::: "memory")
#define WAITLG0() do { __builtin_amdgcn_s_waitcnt(0xC07F); asm volatile("" ::: "memory"); } while (0)
#define SB() __builtin_amdgcn_sched_barrier(0)

constexpr int K1_LDS = 163840;

template <bool Q>
__device__ __forceinline__ void g1_load(lds_char* sm, int lw, int lane, const u16* x, const u16* wqkv, const u16* wo, int rb, int cj, int rot, int pfi, char* ws) {
  constexpr int SL = Q ? 40960 : 34816;
  constexpr int NB = Q ? 6 : 5;
  const rsrc_t rx = mkrsrc(x, S * D * 2), rw = mkrsrc(wqkv, D * 3 * D * 2);
  int va[4], vb[NB];
  {
    const int r8 = lane >> 3;
    const int ab = __umul24(rb * 128 + lw * 32 + r8, 1152);
    const int c0 = ((lane & 7) ^ (r8 >> 1)) << 4;
    va[0] = ab + c0; va[1] = ab + 8 * 1152 + (c0 ^ 64); va[2] = va[0] + 16 * 1152; va[3] = va[1] + 16 * 1152;
  }
  if (Q) {
#pragma unroll
    for (int j = 0; j < 3; ++j) {
      int idx = j * 64 + lane;
      int rho = __umul24(idx, 2731) >> 16, pos = idx - __umul24(rho, 24);
      int b0 = __umul24(lw * 16 + rho, 3456) + cj * 384, sw = (pos ^ (rho & 2)) << 4;
      vb[j] = b0 + sw;
      vb[j + 3] = b0 + 8 * 3456 + (sw ^ 64);
    }
  } else {
    const int u = ((lane >> 1) & 7) ^ (lane >> 4);
    const int b0 = __umul24(lw * 16 + (lane >> 4), 3456) + 1152 + cj * 144 + (u & 2) * 576 + ((lane & 1) << 4) + ((u & 1) << 5);
    vb[0] = b0 + ((u & 4) << 4); vb[1] = vb[0] + 4 * 3456;
    vb[2] = b0 + 8 * 3456 + ((~u & 4) << 4); vb[3] = vb[2] + 4 * 3456;
    const int rho = (lane >> 1) & 15;
    const int kr = (rho & 3) | ((rho & 4) << 1) | ((rho & 8) >> 1);
    vb[4] = __umul24(lw * 16 + kr, 3456) + 1152 + 128 + cj * 144 + (lane & 1) * 1152;
  }
  int st = rot;
  TS(t0);
  unsigned long long ta, tb, tc; u32 dv = 0;
#pragma unroll 1
  for (int it = 0; it < 9; ++it) {
    DG(ta);
    lds_char* base = sm + (it & 3) * SL;
#pragma unroll
    for (int j = 0; j < 4; ++j) dma16(rx, base + (lw * 4 + j) * 1024, va[j], st * 128);
#pragma unroll
    for (int j = 0; j < NB; ++j) {
      if (Q) dma16(rw, base + 16384 + (lw * 6 + j) * 1024, vb[j], st * 64 * 3456);
      else if (j < 4) dma16(rw, base + 16384 + (lw * 4 + j) * 1024, vb[j], st * 64 * 3456);
      else if (lane < 32) dma16(rw, base + 32768 + lw * 512, vb[j], st * 64 * 3456);
    }
    st = st == 8 ? 0 : st + 1;
    DG(tb);
    if (it == 0 || it == 3) { LG0(); DGC(dv, ta, 3 * it); DGC(dv, tb, 3 * it + 1); continue; }
    if (it < 3) { if (Q) WAITVM(10); else WAITVM(9); }
    else { if (Q) WAITVM(20); else WAITVM(18); }
    DG(tc); LG0(); DGC(dv, ta, 3 * it); DGC(dv, tb, 3 * it + 1); DGC(dv, tc, 3 * it + 2);
    if (lw == 0 && it == 1) STAMP(0, 6);
    BARRIER();
  }
  u32 pf = __builtin_amdgcn_raw_buffer_load_b32(mkrsrc(wo, D * D * 2), (pfi * 64 + lane) * 128, 0, 0);
  if (Q) WAITVM(11); else WAITVM(10);
  BARRIER();
  WAITVM(1);
  DG(ta); LG0(); DGC(dv, ta, 27);
  if (lw == 0) { STAMP(0, 7); TSW(0, 5, t0); }
  BARRIER();
  asm volatile("" :: "v"(pf));
  DGW(dv, lw);
}

template <bool Q>
__device__ __forceinline__ void g1_main(lds_char* sm, int w, int lane, f32x4 (&acc)[4][Q ? 6 : 5], char* ws) {
  constexpr int SL = Q ? 40960 : 34816;
  constexpr int NT = Q ? 6 : 5, NS = 2 * NT;
  constexpr int RS = Q ? 384 : 256;
  const int wr = w >> 1, wc = w & 1;
  const int r = lane & 15, g = lane >> 4, q = (lane >> 2) & 3, p = lane & 3;
  const int sw = (r >> 1) & 7;
  lds_char* ao[2][2];
  lds_char* bo[5];
#pragma unroll
  for (int kk = 0; kk < 2; ++kk) {
    int a = (64 * wr + r) * 128 + (((4 * kk + g) ^ sw) << 4);
    ao[kk][0] = sm + (Q ? a : a + 4096 * wc);
    ao[kk][1] = sm + (Q ? a + 4096 : a + 4096 * (1 - wc));
  }
  if (Q) {
    const int f = (q >> 1) | ((g & 1) << 1);
#pragma unroll
    for (int t = 0; t < 4; ++t) {
      int T = 6 * wc + t;
      bo[t] = sm + (16384 + (8 * g + q) * 384 + 8 * p + (((T & 3) ^ f) << 5) + ((T >> 2) << 7));
    }
  } else {
    const int f = q | ((g & 1) << 2);
#pragma unroll
    for (int t = 0; t < 4; ++t) bo[t] = sm + (16384 + (8 * g + q) * 256 + 8 * p + (((4 * wc + t) ^ f) << 5));
    bo[4] = sm + (32768 + (16 * (g >> 1) + 4 * (g & 1) + q) * 32 + 8 * p);
  }
  u32x4 A[2][4];
  u32x2 B[NT][2];
  auto rdA = [&](int kk, int i) {
    A[kk][i] = *(lds_u32x4*)(Q ? ao[kk][0] + 2048 * i : ao[kk][i >> 1] + 2048 * (i & 1));
  };
  auto rdB = [&](int u, int h) {
    const int kk = u / NT, t = u % NT;
    if (!Q && t == 4) B[t][h] = trread(bo[4] + kk * 1024 + h * 256);
    else B[t][h] = trread(bo[t & 3] + (t >> 2) * 128 + kk * 32 * RS + h * 4 * RS);
  };
#pragma unroll
  for (int i = 0; i < 4; ++i)
#pragma unroll
    for (int t = 0; t < NT; ++t)
      if (Q || t < 4 || i < 2) asm volatile("" : "+v"(acc[i][t]));
  asm volatile("" : "+v"(ao[0][0]), "+v"(ao[1][0]), "+v"(bo[0]), "+v"(bo[1]), "+v"(bo[2]), "+v"(bo[3]));
  if (!Q) asm volatile("" : "+v"(ao[0][1]), "+v"(ao[1][1]), "+v"(bo[4]));
  unsigned long long ta, tb; u32 dv = 0;
  DG(ta); LG0(); DGC(dv, ta, 0);
  BARRIER();
  DG(tb);
  rdB(0, 0); rdB(0, 1); SB();
  rdA(0, 0); rdA(0, 1); rdA(0, 2); rdA(0, 3); SB();
#pragma unroll
  for (int u = 1; u < NT - 1; ++u) { rdB(u, 0); rdB(u, 1); SB(); }
  constexpr int MB = Q ? 31 : 25;
#pragma unroll 1
  for (int st = 0; st < 9; ++st) {
    const int dl = (st & 3) == 3 ? -3 * SL : SL;
    __builtin_amdgcn_s_waitcnt(0xC27F);
#pragma unroll
    for (int u = 0; u < NS; ++u) {
      const int kk = u / NT, t = u % NT;
#pragma unroll
      for (int i = 0; i < 4; ++i) {
        if (!Q && t == 4 && i >= 2) continue;
        const int m = Q ? 4 * u + i : 18 * kk + 4 * t + i;
        const u32x4 Bf = cat(B[t][0], B[t][1]);
        acc[i][t] = Q ? mfma(Bf, A[kk][i], acc[i][t]) : mfma(A[kk][i], Bf, acc[i][t]);
        SB();
        if (m < 2) rdB(NT - 1, m);
        else if (m == 2 || m == 3) rdA(1, m - 2);
        else if (m == 6 || m == 7) rdA(1, m - 4);
        else if (u >= 1 && u <= NT && i < 2) rdB(u + NT - 1, i);
        else if (m == MB - 5) { ao[0][0] += dl; if (Q) ao[1][0] += dl; else ao[0][1] += dl; }
        else if (m == MB - 4) { bo[0] += dl; bo[1] += dl; }
        else if (m == MB - 3) { bo[2] += dl; bo[3] += dl; }
        else if (m == MB - 2 && !Q) { ao[1][0] += dl; ao[1][1] += dl; }
        else if (m == MB - 1 && !Q) { bo[4] += dl; }
        else if (m == MB) {
          DG(ta);
          WAITLG0();
          DGC(dv, tb, 1 + 2 * st); DGC(dv, ta, 2 + 2 * st);
          BARRIER();
          DG(tb);
        }
        else if (m == MB + 1) { rdB(0, 0); rdB(0, 1); }
        else if (m == MB + 2) { rdA(0, 0); rdA(0, 1); }
        else if (m == MB + 3) { rdA(0, 2); rdA(0, 3); }
        else if (m == MB + 4) { rdB(1, 0); rdB(1, 1); }
        else if (u >= NT + 3 && i < 2) rdB(u - NT - 1, i);
        SB();
      }
    }
  }
  DG(ta); LG0(); DGC(dv, tb, 19); DGC(dv, ta, 20);
  DGW(dv, 4 + w);
}

constexpr int TROW = 544;
constexpr int TBW = 13632;
constexpr int UOFF = 8 * TBW;
constexpr int UW = 4608;
constexpr int MOFF = UOFF + 8 * UW;
constexpr int XG = 2320, XW = 9280;
__device__ __forceinline__ u32 mktag(u32 e) { return e * 0x9E3779B1u + 0x7F4A7C15u; }

__global__ __launch_bounds__(512, 1) void k1(const u16* __restrict__ x, const u16* __restrict__ wqkv, const u16* __restrict__ wo,
                                             const u16* __restrict__ Are, const u16* __restrict__ Aim,
                                             const u16* __restrict__ Bre, const u16* __restrict__ Bim,
                                             const u16* __restrict__ Cre, const u16* __restrict__ Cim,
                                             const u16* __restrict__ logdt, const u16* __restrict__ dskip,
                                             const u16* __restrict__ gw, char* __restrict__ ws) {
  extern __shared__ __attribute__((aligned(16))) char shm_[];
  lds_char* sm = (lds_char*)shm_;
  const int tid = threadIdx.x, lane = tid & 63;
  const int w = __builtin_amdgcn_readfirstlane(tid >> 6);
  const int xcd = blockIdx.x & 7, slot = blockIdx.x >> 3;
  const int c = lane & 15, g = lane >> 4;
  const f32x4 z4 = {0.f, 0.f, 0.f, 0.f};
  if (w == 0) STAMP(0, 0);
  if (__builtin_expect(slot < 16 && w < 4, 1)) {
    g1_load<false>(sm, w, lane, x, wqkv, wo, (xcd >> 1) * 4 + (slot >> 2), (xcd & 1) * 4 + (slot & 3), (slot * 5) % 9, slot * 4 + w, ws);
    if (w != 0) return;
    const int rid = xcd * 16 + slot, head = rid >> 2, qt = rid & 3;
    const u32 tag = mktag(*(const u32*)(ws + OFF_EP + rid * 4));
    const int l4 = lane >> 2, gg = (l4 * 13) >> 6, e5 = l4 - 5 * gg;
    int ip = 4 * gg + (lane & 3) - (head & 3);
    const int e = 5 * qt + e5;
    const bool act = (unsigned)ip < 9u && e < 19 && lane < 60;
    const int vo = act ? (ip * 5 + e5) * 16 : OOB;
    const rsrc_t rk = mkrsrc(ws, OFF_END);
    const int so = OFF_KVP + head * 16 * 3072 + qt * 768;
    BARRIER();
    BARRIER();
    __builtin_amdgcn_s_sleep(12);
    STAMP(0, 11);
    float s0, s1;
#pragma unroll 1
    for (int sp = 0; sp < 20000; ++sp) {
      u32x4 v[16];
      asm volatile("" ::: "memory");
#pragma unroll
      for (int i = 0; i < 16; ++i) v[i] = __builtin_amdgcn_raw_buffer_load_b128(rk, vo, so + i * 3072, 16);
      u32 bad = 0; s0 = 0.f; s1 = 0.f;
#pragma unroll
      for (int i = 0; i < 16; ++i) {
        bad |= (v[i].y ^ tag) | (v[i].w ^ tag);
        s0 += __uint_as_float(v[i].x); s1 += __uint_as_float(v[i].z);
      }
      if (__builtin_amdgcn_ballot_w64(bad != 0 && act) == 0) break;
    }
    const u32 r0 = pk2(s0, s1);
    const u32x4 r = {r0, (u32)__builtin_amdgcn_mov_dpp((int)r0, 0x55, 0xf, 0xf, true), (u32)__builtin_amdgcn_mov_dpp((int)r0, 0xaa, 0xf, 0xf, true), (u32)__builtin_amdgcn_mov_dpp((int)r0, 0xff, 0xf, 0xf, true)};
    st16(r, rk, (lane & 3) == 0 && e < 19 && lane < 60 ? e * 48 + gg * 16 : OOB, OFF_KVT + head * 1024);
    __builtin_amdgcn_raw_buffer_store_b32(tag, rk, lane == 0 ? rid * 4 : OOB, OFF_EP, 16);
    STAMP(0, 12);
    return;
  }
  if (__builtin_expect(slot < 22 && w < 4, 1)) {
    g1_load<true>(sm, w, lane, x, wqkv, wo, 2 * xcd + (slot - 16) / 3, (slot - 16) % 3, (slot * 5) % 9, slot * 4 + w, ws);
    return;
  }
  if (slot < 16) {
    const int rb = (xcd >> 1) * 4 + (slot >> 2), kvj = (xcd & 1) * 4 + (slot & 3);
    const int cw = w - 4, wr = cw >> 1, wc = cw & 1;
    const int e = lane & 31, hi = lane >> 5, qt = (e * 13) >> 6;
    const u32 tag = mktag(*(const u32*)(ws + OFF_EP + (kvj * 4 + cw) * 16 + (qt & 3) * 4));
    f32x4 acc[4][5];
    const float i9 = c < 8 ? 1.f : 0.f;
#pragma unroll
    for (int i = 0; i < 4; ++i) {
      acc[i][0] = acc[i][1] = (f32x4){1.f, 1.f, 1.f, 1.f};
      acc[i][2] = acc[i][3] = z4;
      acc[i][4] = (f32x4){i9, i9, i9, i9};
    }
    g1_main<false>(sm, cw, lane, acc, ws);
    if (cw == 0) STAMP(0, 1);
    lds_char* X = sm + 34816;
    {
      lds_char* x0 = X + (2 * wr + wc) * XW + g * XG + c * 16;
      lds_char* x1 = X + (2 * wr + 1 - wc) * XW + g * XG + c * 16;
#pragma unroll
      for (int ip = 0; ip < 2; ++ip) {
        lds_char* xb = (ip ? x1 : x0) + wc * 512;
#pragma unroll
        for (int t = 2; t < 4; ++t) {
          f32x4 a = acc[2 * ip][t], b = acc[2 * ip + 1][t];
          *(lds_u32x4*)(xb + 1152 + (t - 2) * 256) = (u32x4){pk2(a[0], a[1]), pk2(a[2], a[3]), pk2(b[0], b[1]), pk2(b[2], b[3])};
        }
      }
      if (lane < 4) *(lds_u32x4*)(X + cw * XW + lane * XG + 2304) = (u32x4){0x3f803f80u, 0x3f803f80u, 0x3f803f80u, 0x3f803f80u};
#pragma unroll
      for (int ip = 0; ip < 2; ++ip) {
        lds_char* xb = (ip ? x1 : x0) + wc * 512;
#pragma unroll
        for (int t = 0; t < 2; ++t) {
          f32x4 a = acc[2 * ip][t], b = acc[2 * ip + 1][t];
          *(lds_u32x4*)(xb + t * 256) = (u32x4){phi2(a[0], a[1]), phi2(a[2], a[3]), phi2(b[0], b[1]), phi2(b[2], b[3])};
        }
      }
      {
        f32x4 a = acc[0][4], b = acc[1][4];
        u32x4 pk = {phi2(a[0], a[1]), phi2(a[2], a[3]), phi2(b[0], b[1]), phi2(b[2], b[3])};
        u32x4 pv = {pk2(a[0], a[1]), pk2(a[2], a[3]), pk2(b[0], b[1]), pk2(b[2], b[3])};
        *(lds_u32x4*)(x0 + (c < 8 ? 1024 : 2048)) = c < 8 ? pk : pv;
      }
    }
    lds_char* pa = X + hi * XG + (16 * cw + e) * 16;
    lds_char* pb = X + hi * XG + (e < 18 ? 72 + 18 * cw + e : 144) * 16;
    const int vb = qt * 768 + (e - 5 * qt) * 16 + (2 * hi - cw) * 80;
    int vo[6];
#pragma unroll
    for (int k = 0; k < 6; ++k) {
      const int i0 = 4 * (k >> 1) + (k & 1);
      vo[k] = (unsigned)(i0 + 2 * hi - cw) < 9u && e < 19 ? vb + i0 * 80 : OOB;
    }
    WAITLG0();
    BARRIER();
    if (cw == 0) STAMP(0, 2);
    {
      u32x4 fa[8], fb[8];
#pragma unroll
      for (int m = 0; m < 8; ++m) {
        fa[m] = *(lds_u32x4*)(pa + (m >> 1) * XW + (m & 1) * 2 * XG);
        fb[m] = *(lds_u32x4*)(pb + (m >> 1) * XW + (m & 1) * 2 * XG);
      }
      f32x16 kv = {0.f, 0.f, 0.f, 0.f, 0.f, 0.f, 0.f, 0.f, 0.f, 0.f, 0.f, 0.f, 0.f, 0.f, 0.f, 0.f};
#pragma unroll
      for (int m = 0; m < 8; ++m) kv = mfma32(fa[m], fb[m], kv);
      const rsrc_t rk = mkrsrc(ws, OFF_END);
      const int so = OFF_KVP + ((kvj * 4 + cw) * 16 + rb) * 3072;
#pragma unroll
      for (int k = 0; k < 6; ++k)
        st16((u32x4){__float_as_uint(kv[2 * k]), tag, __float_as_uint(kv[2 * k + 1]), tag}, rk, vo[k], so);
    }
    if (cw == 0) STAMP(0, 3);
    return;
  }
  if (slot < 22) {
    const int rb = 2 * xcd + (slot - 16) / 3, qj = (slot - 16) % 3;
    const int cw = w - 4, wr = cw >> 1, wc = cw & 1;
    f32x4 acc[4][6];
#pragma unroll
    for (int i = 0; i < 4; ++i)
#pragma unroll
      for (int t = 0; t < 6; ++t) acc[i][t] = (f32x4){1.f, 1.f, 1.f, 1.f};
    g1_main<true>(sm, cw, lane, acc, ws);
    if (cw == 0) STAMP(0, 8);
    const rsrc_t rk = mkrsrc(ws, OFF_END);
    const int so = OFF_PQ + (rb * 8 + wr * 4) * 18432 + (24 * qj + 12 * wc) * 256;
    const int vo = (2 * (g & 1) + (g >> 1)) * 256 + c * 16;
#pragma unroll
    for (int i = 0; i < 4; ++i)
#pragma unroll
      for (int t = 0; t < 6; t += 2) {
        f32x4 a = acc[i][t], b = acc[i][t + 1];
        u32x4 v = swap16((u32x2){phi2(a[0], a[1]), phi2(a[2], a[3])}, (u32x2){phi2(b[0], b[1]), phi2(b[2], b[3])});
        st16(v, rk, vo + t * 512, so + i * 18432);
      }
    WAITVM(0);
    {
      const int l2 = lane < 48 ? (lane >= 24 ? 18432 - 3072 : 0) + lane * 128 : OOB;
      u32 p0 = __builtin_amdgcn_raw_buffer_load_b32(rk, l2, so, 0), p1 = __builtin_amdgcn_raw_buffer_load_b32(rk, l2, so + 2 * 18432, 0);
      asm volatile("" :: "v"(p0), "v"(p1));
    }
    if (cw == 0) STAMP(0, 9);
    return;
  }
  if (slot < 31) {
    const int ci = slot - 22;
    const int chunk = ci < 8 ? 8 * xcd + ci : 64 + xcd;
    const int d = chunk * 8 + w;
    const u32 vAre = *(const u32*)(Are + d * 128 + 2 * lane), vAim = *(const u32*)(Aim + d * 128 + 2 * lane);
    const u32 vBre = *(const u32*)(Bre + d * 128 + 2 * lane), vBim = *(const u32*)(Bim + d * 128 + 2 * lane);
    const u32 vCre = *(const u32*)(Cre + d * 128 + 2 * lane), vCim = *(const u32*)(Cim + d * 128 + 2 * lane);
    const u16 vld = logdt[d];
    const u16 vds = dskip[d];
    if (w >= 4) {
      const int y = w - 4;
      const char* xs = (const char*)x + (size_t)(y * 512 + 4 * lane) * 1152 + chunk * 16;
      u32x4 xv[2][4];
#pragma unroll
      for (int i = 0; i < 2; ++i)
#pragma unroll
        for (int j = 0; j < 4; ++j) xv[i][j] = *(const u32x4*)(xs + (i * 256 + j) * 1152);
      __builtin_amdgcn_s_setprio(1);
      if ((lane & 31) < 30) *(lds_u32x4*)(sm + UOFF + (2 * y + (lane >> 5)) * UW + (lane & 31) * 16) = (u32x4){0u, 0u, 0u, 0u};
#pragma unroll
      for (int mm = 0; mm < 2; ++mm) {
        const int K = ((mm ? 0x0c0c1312 : 0x0c14150d) >> (8 * y)) & 255, len = ((mm ? 0x2275 : 0x2933) >> (4 * y)) & 15;
        const u32 V = (mm && y == 2) ? 0xffffu : 0x3f80u;
        const u32 bits = (u32)(((unsigned long long)((1u << len) - 1u) << (c + 8 * g + 24 - K)) >> 24);
        u32x4 r;
#pragma unroll
        for (int i = 0; i < 4; ++i) r[i] = __umul24(__umul24((bits >> (2 * i)) & 3u, 0x8001u) & 0x10001u, V);
        *(lds_u32x4*)(sm + MOFF + (2 * y + mm) * 1024 + lane * 16) = r;
      }
      lds_char* ub = sm + UOFF + 480 + y * 1024 + lane * 8;
#pragma unroll
      for (int i = 0; i < 2; ++i)
#pragma unroll
        for (int k = 0; k < 4; ++k) {
          *(lds_u32x2*)(ub + i * 512 + (2 * k) * UW) = (u32x2){__builtin_amdgcn_perm(xv[i][1][k], xv[i][0][k], 0x05040100u), __builtin_amdgcn_perm(xv[i][3][k], xv[i][2][k], 0x05040100u)};
          *(lds_u32x2*)(ub + i * 512 + (2 * k + 1) * UW) = (u32x2){__builtin_amdgcn_perm(xv[i][1][k], xv[i][0][k], 0x07060302u), __builtin_amdgcn_perm(xv[i][3][k], xv[i][2][k], 0x07060302u)};
        }
      WAITLG0();
      __builtin_amdgcn_s_setprio(0);
      if (w == 4) STAMP(0, 10);
      BARRIER();
    }
    lds_char* tV = sm + w * TBW;
    lds_char* si = tV + 16 * TROW;
    {
      const float dt = lo16(pk2(__builtin_amdgcn_exp2f(lo16(vld) * 1.44269504088896f), 0.f));
      f32x2 w1[2], w16[2], w256[2], cb[2];
#pragma unroll
      for (int m = 0; m < 2; ++m) {
        float ar = dt * (m ? hi16(vAre) : lo16(vAre)), ai = dt * (m ? hi16(vAim) : lo16(vAim));
        float br = dt * (m ? hi16(vBre) : lo16(vBre)), bi = dt * (m ? hi16(vBim) : lo16(vBim));
        float cr = m ? hi16(vCre) : lo16(vCre), cim = m ? hi16(vCim) : lo16(vCim);
        cb[m] = (f32x2){cr * br - cim * bi, -(cr * bi + cim * br)};
        float le = ar * 1.44269504088896f, rv = ai * 0.15915494309189535f;
        float e1 = __builtin_amdgcn_exp2f(le), e16 = __builtin_amdgcn_exp2f(le * 16.f), e256 = __builtin_amdgcn_exp2f(le * 256.f);
        float r1 = __builtin_amdgcn_fractf(rv), r16 = __builtin_amdgcn_fractf(rv * 16.f), r256 = __builtin_amdgcn_fractf(rv * 256.f);
        w1[m] = (f32x2){e1 * __builtin_amdgcn_cosf(r1), -e1 * __builtin_amdgcn_sinf(r1)};
        w16[m] = (f32x2){e16 * __builtin_amdgcn_cosf(r16), e16 * __builtin_amdgcn_sinf(r16)};
        w256[m] = (f32x2){e256 * __builtin_amdgcn_cosf(r256), -e256 * __builtin_amdgcn_sinf(r256)};
      }
      auto put = [&](lds_char* t, int row, f32x2 a, f32x2 b) {
        *(lds_u32x2*)(t + row * TROW + lane * 8) = (u32x2){pk2(a.x, a.y), pk2(b.x, b.y)};
      };
      f32x2 ko[4][2];
      {
        f32x2 v0 = w16[0], v1 = w16[1];
        f32x2 w2[2] = {cmul(w1[0], w1[0]), cmul(w1[1], w1[1])};
        f32x2 u0 = cmul(cb[0], w256[0]), u1 = cmul(cb[1], w256[1]);
        *(lds_u32x2*)(tV + lane * 8) = (u32x2){0x00003f80u, 0x00003f80u};
        put(tV, 1, v0, v1);
        put(si, 8, u0, u1);
#pragma unroll
        for (int e = 2; e < 16; ++e) {
          v0 = cmul(v0, w16[0]); v1 = cmul(v1, w16[1]); put(tV, e, v0, v1);
          if (e < 10) {
            u0 = cmul(u0, w2[0]); u1 = cmul(u1, w2[1]);
            put(si, e - 2, u0, u1);
            if (e & 1) { ko[(e - 2) >> 1][0] = u0; ko[(e - 2) >> 1][1] = u1; }
          }
        }
      }
      const int fr = c * TROW + g * 16;
      u32x4 Vf[8];
      f32x4 t0 = z4, ta = z4, tbb = z4;
#pragma unroll
      for (int kk = 0; kk < 8; ++kk) {
        Vf[kk] = *(lds_u32x4*)(tV + fr + kk * 64);
        u32x4 af = *(lds_u32x4*)(si + fr + kk * 64);
        ta = mfma(af, Vf[kk], ta);
      }
      {
        f32x2 u0 = cb[0], u1 = cb[1];
        put(tV, 0, u0, u1);
#pragma unroll
        for (int e = 1; e < 16; ++e) { u0 = cmul(u0, w1[0]); u1 = cmul(u1, w1[1]); put(tV, e, u0, u1); }
      }
#pragma unroll
      for (int kk = 0; kk < 8; ++kk) t0 = mfma(*(lds_u32x4*)(tV + fr + kk * 64), Vf[kk], t0);
      const u32x4 zz = {0u, 0u, 0u, 0u};
      *(lds_u32x4*)(si + lane * 16) = zz;
      *(lds_u32x4*)(si + 1024 + lane * 16) = zz;
      if (lane < 4) *(lds_u32x4*)(si + 2048 + lane * 16) = zz;
#pragma unroll
      for (int o = 0; o < 4; ++o) {
        f32x2 a0 = cmul(ko[o][0], w256[0]), a1 = cmul(ko[o][1], w256[1]);
        put(tV, o, a0, a1);
        a0 = cmul(a0, w256[0]); a1 = cmul(a1, w256[1]);
        put(tV, 4 + o, a0, a1);
        if (o & 1) {
#pragma unroll
          for (int qq = 0; qq < 4; ++qq) {
            a0 = cmul(a0, w256[0]); a1 = cmul(a1, w256[1]);
            put(tV, 8 + 2 * qq + (o >> 1), a0, a1);
          }
        }
      }
#pragma unroll
      for (int kk = 0; kk < 8; ++kk) tbb = mfma(Vf[kk], *(lds_u32x4*)(tV + fr + kk * 64), tbb);
      *(lds_u32x2*)(si + 2 * (540 - 16 * c - 4 * g)) = (u32x2){pk2(t0[3], t0[2]), pk2(t0[1], t0[0])};
      if (g < 2) {
        lds_u32* pR = (lds_u32*)(si + 2 * (278 - 16 * c - 8 * g));
        pR[0] = pk2(ta[3], ta[3]); pR[1] = pk2(ta[3], ta[2]); pR[2] = pk2(ta[1], ta[1]); pR[3] = pk2(ta[1], ta[0]);
        if (lane == 31) *(lds_u16*)(si + 2 * 29) = (u16)pk2(ta[3], ta[3]);
      } else if (lane == 32) {
        *(lds_u32*)(si + 2 * 286) = pk2(ta[0], ta[0]);
      }
      {
        int p0 = c < 8 ? 560 + 64 * (c & 3) + 44 - 16 * (c >> 2) - 4 * g : 816 + 80 * (c & 1) + 60 - 16 * ((c - 8) >> 1) - 4 * g;
        *(lds_u32x2*)(si + 2 * p0) = (u32x2){pk2(tbb[3], tbb[2]), pk2(tbb[1], tbb[0])};
      }
#pragma unroll
      for (int i = 0; i < 2; ++i) {
        u32x4 a = *(lds_u32x4*)(si + i * 1024 + lane * 16);
        u32 n = *(lds_u32*)(si + i * 1024 + lane * 16 + 16);
        *(lds_u32x4*)(si + 2112 + i * 1024 + lane * 16) = (u32x4){__builtin_amdgcn_alignbit(a.y, a.x, 16), __builtin_amdgcn_alignbit(a.z, a.y, 16), __builtin_amdgcn_alignbit(a.w, a.z, 16), __builtin_amdgcn_alignbit(n, a.w, 16)};
      }
    }
    if (w == 0) STAMP(0, 13);
    if (w == 7) STAMP(0, 14);
    if (w < 4) BARRIER();
    lds_char* su = sm + UOFF + w * UW;
    lds_char* ub = su + 32 + 32 * c + 16 - 16 * g;
    lds_char* kb = si + ((c & 1) ? 2 * (551 - 8 * g - c) : 2112 + 2 * (550 - 8 * g - c)) - 960;
    lds_char* k16 = kb - 64;
    lds_char* sb = si + ((c & 1) ? 2 * (79 - c + 4 * g) : 2112 + 2 * (78 - c + 4 * g)) + 2 * 496;
    lds_char* mb = sm + MOFF + lane * 16;
    asm volatile("" : "+v"(ub), "+v"(kb), "+v"(k16), "+v"(sb));
    auto ufrag = [&](int s) { return *(lds_u32x4*)(ub + 448 + 64 * s); };
    auto kfrag = [&](int i) { lds_u32* pp = (lds_u32*)(i == 16 ? k16 : kb + 960 - 64 * i); return (u32x4){pp[0], pp[1], pp[2], pp[3]}; };
    auto sfrag = [&](int e0, int e1) { lds_u32* pp = (lds_u32*)(sb + 2 * (e0 - 496)); lds_u32* q2 = (lds_u32*)(sb + 2 * (e1 - 496)); return (u32x4){pp[0], pp[1], q2[0], q2[1]}; };
    f32x4 acc[8];
    {
      u32x4 U[8], F2[6][2], F3[4];
#pragma unroll
      for (int m = 0; m < 8; ++m) U[m] = ufrag(8 * m);
      u32x4 A0 = kfrag(0), A1 = kfrag(8), A2 = kfrag(16);
      const u32x4 bd0 = *(lds_u32x4*)(mb), bd1 = *(lds_u32x4*)(mb + 1024), bd2 = *(lds_u32x4*)(mb + 2048), bd4 = *(lds_u32x4*)(mb + 3072), bd5 = *(lds_u32x4*)(mb + 4096), fixm = *(lds_u32x4*)(mb + 5120);
      u32 k1024 = *(lds_u16*)(si + 2 * (560 + 192 + 16));
      k1024 |= k1024 << 16;
#pragma unroll
      for (int T = 0; T < 8; ++T) acc[T] = mfma(A0, U[T], z4);
#pragma unroll
      for (int m = 0; m < 6; ++m) {
        u32x4 Us = *(lds_u32x4*)(ub + 448 + 64 * 8 * m - 16);
        f32x4 f0 = mfma(U[m], bd0, z4), f1 = mfma(U[m], bd1, z4), f2 = mfma(U[m], bd2, z4), f3 = mfma(Us, bd1, z4);
        F2[m][0] = (u32x4){pk2(f0[0], f0[1]), pk2(f0[2], f0[3]), pk2(f1[0], f1[1]), pk2(f1[2], f1[3])};
        F2[m][1] = (u32x4){pk2(f2[0], f2[1]), pk2(f2[2], f2[3]), pk2(f3[0], f3[1]), pk2(f3[2], f3[3])};
        if (m < 4) {
          f32x4 f4 = mfma(U[m], bd4, z4), f5 = mfma(Us, bd5, z4);
          F3[m] = (u32x4){pk2(f4[0], f4[1]), pk2(f4[2], f4[3]), pk2(f5[0], f5[1]), pk2(f5[2], f5[3])};
        }
      }
#pragma unroll
      for (int T = 1; T < 8; ++T) acc[T] = mfma(A1, U[T - 1], acc[T]);
#pragma unroll
      for (int T = 2; T < 8; ++T) acc[T] = mfma(A2, U[T - 2], acc[T]);
      u32x4 fx = {fixm[0] & k1024, fixm[1] & k1024, fixm[2] & k1024, fixm[3] & k1024};
#pragma unroll
      for (int T = 4; T < 8; ++T) acc[T] = mfma(fx, U[T - 4], acc[T]);
#pragma unroll
      for (int dl = 2; dl <= 4; ++dl)
#pragma unroll
        for (int pr = 0; pr < 2; ++pr) {
          u32x4 Sf = sfrag(560 + 128 * pr - 16 * dl, 560 + 128 * pr + 64 - 16 * dl);
#pragma unroll
          for (int T = dl; T < 8; ++T) acc[T] = mfma(F2[T - dl][pr], Sf, acc[T]);
        }
#pragma unroll
      for (int dl = 4; dl < 8; ++dl) {
        u32x4 Sf = sfrag(816 + 48 - 16 * dl, 816 + 80 + 48 - 16 * dl);
#pragma unroll
        for (int T = dl; T < 8; ++T) acc[T] = mfma(F3[T - dl], Sf, acc[T]);
      }
    }
#pragma unroll
    for (int r = 1; r < 8; ++r) {
      u32x4 A0 = kfrag(r), A1 = kfrag(8 + r);
      u32x4 Up;
#pragma unroll
      for (int T = 0; T < 8; ++T) {
        u32x4 U = ufrag(8 * T - r);
        if (T > 0) acc[T] = mfma(A1, Up, acc[T]);
        acc[T] = mfma(A0, U, acc[T]);
        Up = U;
      }
    }
    if (w == 0) STAMP(0, 15);
    const float dsk = lo16(vds);
    const rsrc_t rk = mkrsrc(ws, OFF_END);
    const int vo = 512 * (g & 1) + 32 * c + 16 * (g >> 1);
    u32x2 o[8];
#pragma unroll
    for (int T = 0; T < 8; ++T) {
      u32x2 uu = *(lds_u32x2*)(su + 480 + 512 * T + 32 * c + 8 * g);
      o[T] = (u32x2){pk2(fmaf(lo16(uu.x), dsk, acc[T][0]), fmaf(hi16(uu.x), dsk, acc[T][1])), pk2(fmaf(lo16(uu.y), dsk, acc[T][2]), fmaf(hi16(uu.y), dsk, acc[T][3]))};
      if (T & 1) st16(swap16(o[T - 1], o[T]), rk, vo + 512 * (T - 1), OFF_BT + d * 4096);
    }
    if (w == 0) STAMP(0, 16);
    if (w == 7) STAMP(0, 17);
    return;
  }
  {
    const int row = 256 * xcd + 32 * w + c;
    const char* xs = (const char*)x + (size_t)row * 1152 + g * 16;
    u32x4 gf[18], xf[18], yf[18];
#pragma unroll
    for (int k = 0; k < 18; ++k) { xf[k] = *(const u32x4*)(xs + k * 64); gf[k] = *(const u32x4*)((const char*)gw + k * 64 + g * 16); yf[k] = *(const u32x4*)(xs + 16 * 1152 + k * 64); }
    SB();
    f32x4 a0 = z4, a1 = z4;
#pragma unroll
    for (int k = 0; k < 18; ++k) { a0 = mfma(gf[k], xf[k], a0); a1 = mfma(gf[k], yf[k], a1); }
    const float gs = lo16(pk2(g == 0 ? a0[0] : a1[0], 0.f));
    const float gg = lo16(pk2(__builtin_amdgcn_rcpf(1.f + __builtin_amdgcn_exp2f(-gs * 1.44269504088896f)), 0.f));
    __builtin_amdgcn_raw_buffer_store_b32(pk2(gg, 1.f - gg), mkrsrc(ws, OFF_END), g < 2 ? (row + 16 * g) * 4 : OOB, OFF_G, 16);
    if (w == 0) STAMP(0, 18);
  }
}

constexpr int L_BT = 576 * 144, L_R = L_BT + 9216, K2_LDS = L_R + 54 * 1024;

__global__ __launch_bounds__(512, 1) void k2(const u16* __restrict__ wo, char* __restrict__ ws, u16* __restrict__ out) {
  extern __shared__ __attribute__((aligned(16))) char shm_[];
  lds_char* sm = (lds_char*)shm_;
  const int tid = threadIdx.x, lane = tid & 63;
  const int w = __builtin_amdgcn_readfirstlane(tid >> 6);
  const int xcd = blockIdx.x & 7, slot = blockIdx.x >> 3;
  const int row0 = 256 * xcd + 64 * (slot >> 3), cg = slot & 7;
  const int rp = w & 1, hq = w >> 1;
  const int c = lane & 15, g = lane >> 4, q = (lane >> 2) & 3, p4 = lane & 3;
  const rsrc_t rs = mkrsrc(ws, OFF_END);
  if (w == 0) STAMP(1, 0);
  u32x4 KE[8], KX[4], P[2][8];
  {
    const int vk = g < 3 ? c * 48 + g * 16 : OOB;
    const int vx = g < 3 ? (p4 < 2 ? p4 * 1024 + 18 * 48 : (q & 1) * 1024 + (14 + p4) * 48) + g * 16 : OOB;
    const int vp = g < 3 ? lane * 16 : OOB;
    const int sk = OFF_KVT + hq * 8192, sp = OFF_PQ + (row0 / 16 + 2 * rp) * 18432 + hq * 4608;
#pragma unroll
    for (int p = 0; p < 4; ++p) {
      KX[p] = __builtin_amdgcn_raw_buffer_load_b128(rs, vx, sk + p * 2048, 0);
      KE[2 * p] = __builtin_amdgcn_raw_buffer_load_b128(rs, vk, sk + p * 2048, 0);
      KE[2 * p + 1] = __builtin_amdgcn_raw_buffer_load_b128(rs, vk + 1024, sk + p * 2048, 0);
    }
#pragma unroll
    for (int h = 0; h < 8; ++h) {
      P[0][h] = __builtin_amdgcn_raw_buffer_load_b128(rs, vp + ((9 * h) >> 2) * 256, sp, 0);
      P[1][h] = __builtin_amdgcn_raw_buffer_load_b128(rs, vp + ((9 * h) >> 2) * 256, sp + 18432, 0);
    }
  }
  SB();
  {
    const rsrc_t rw = mkrsrc(wo, D * D * 2);
    int r = (lane * 57) >> 9, ch = lane - 9 * r;
    int vo = r * 1152 + ch * 16;
#pragma unroll
    for (int j = 0; j < 11; ++j) {
      if (j < 10 || lane < 8) dma16(rw, sm + w * 10368 + j * 1024, vo, w * 72 * 1152 + cg * 144 + j * (7 * 1152 + 16));
      if (j < 9) vo += ch == 8 - j ? 1152 - 144 : 0;
      if (j == 9) vo += ch == 8 ? 1152 - 144 : 0;
    }
    const int dl = lane >> 3;
    const int vb = dl * 4096 + (((lane & 7) ^ (dl & 6)) << 4);
    dma16(rs, sm + L_BT + w * 1024, vb, OFF_BT + (cg * 72 + w * 8) * 4096 + row0 * 2);
    if (w == 7) dma16(rs, sm + L_BT + 8192, vb, OFF_BT + (cg * 72 + 64) * 4096 + row0 * 2);
  }
  const int ort = 2 * rp + (hq >> 1);
  const u32 gv = __builtin_amdgcn_raw_buffer_load_b32(rs, c * 4, OFF_G + (row0 + 16 * ort) * 4, 0);
  const u32 gv4 = __builtin_amdgcn_raw_buffer_load_b32(rs, (c + (g >> 1) * 16) * 4, OFF_G + (row0 + 32 * rp) * 4, 0);
  SB();
  const f32x4 z4 = {0.f, 0.f, 0.f, 0.f}, eps4 = {1e-6f, 1e-6f, 0.f, 0.f};
  u32x4 Ob[2][4];
  u32x2 Xb[2];
#pragma unroll
  for (int p = 0; p < 4; ++p)
#pragma unroll
    for (int rt = 0; rt < 2; ++rt) {
      f32x4 X0 = mfma(KX[p], P[rt][2 * p], eps4), X1 = mfma(KX[p], P[rt][2 * p + 1], eps4);
      f32x4 E0 = mfma(KE[2 * p], P[rt][2 * p], z4), E1 = mfma(KE[2 * p + 1], P[rt][2 * p + 1], z4);
      const float r0 = __builtin_amdgcn_rcpf(X0[0]), r1 = __builtin_amdgcn_rcpf(X1[1]);
      E0 *= r0; E1 *= r1;
      Ob[rt][p] = (u32x4){pk2(E0[0], E0[1]), pk2(E0[2], E0[3]), pk2(E1[0], E1[1]), pk2(E1[2], E1[3])};
      const u32 x0 = pk2(X0[2] * r0, X0[3] * r0), x1 = pk2(X1[2] * r1, X1[3] * r1);
      const u32 xv = (g & 1) ? x1 : x0;
      if (p & 1) Xb[rt][p >> 1] = g >= 2 ? xv : Xb[rt][p >> 1]; else Xb[rt][p >> 1] = xv;
    }
  if (w == 0) STAMP(1, 1);
  lds_char* aw = sm + (144 * hq + 4 * g + q) * 144 + 8 * p4;
  lds_char* ax = sm + (144 * hq + 72 * (q >> 1) + 36 * (g >> 1) + 18 * (g & 1) + 16 + (q & 1)) * 144 + 8 * p4;
  SB();
  WAITVM(0);
  BARRIER();
  SB();
  if (w == 0) STAMP(1, 2);
  f32x4 acc[2][5];
#pragma unroll
  for (int t = 0; t < 5; ++t) {
    u32x2 wx = trread(ax + 32 * t);
    acc[0][t] = mfma16(wx, Xb[0], z4);
    acc[1][t] = mfma16(wx, Xb[1], z4);
  }
#pragma unroll
  for (int p = 0; p < 4; ++p)
#pragma unroll
    for (int t = 0; t < 5; ++t) {
      u32x4 wf = cat(trread(aw + 36 * 144 * p + 32 * t), trread(aw + 36 * 144 * p + 18 * 144 + 32 * t));
      acc[0][t] = mfma(wf, Ob[0][p], acc[0][t]);
      acc[1][t] = mfma(wf, Ob[1][p], acc[1][t]);
    }
  if (w == 0) STAMP(1, 3);
  f32x4 a4;
#pragma unroll
  for (int i = 0; i < 4; ++i) {
    auto r = __builtin_amdgcn_permlane32_swap(__float_as_uint(acc[0][4][i]), __float_as_uint(acc[1][4][i]), false, false);
    a4[i] = __uint_as_float(r[0]);
  }
  lds_char* rw_ = sm + L_R + rp * 27648 + lane * 16;
  f32x4 o0, o1;
#pragma unroll
  for (int o = 0; o < 4; ++o) {
    if (o == hq) { o0 = acc[o >> 1][2 * (o & 1)]; o1 = acc[o >> 1][2 * (o & 1) + 1]; }
    else {
      lds_char* dp = rw_ + o * 6144 + ((hq - o - 1) & 3) * 2048;
      *(lds_u32x4*)(dp) = __builtin_bit_cast(u32x4, acc[o >> 1][2 * (o & 1)]);
      *(lds_u32x4*)(dp + 1024) = __builtin_bit_cast(u32x4, acc[o >> 1][2 * (o & 1) + 1]);
    }
  }
  if (hq != 3) *(lds_u32x4*)(rw_ + 24576 + hq * 1024) = __builtin_bit_cast(u32x4, a4);
  const int f = (q >> 1) | ((g & 1) << 1);
  lds_char* bb = sm + L_BT + (32 * (hq & 1) + 4 * g + q) * 128 + ((ort ^ f) << 5) + 8 * p4;
  lds_char* bb4 = sm + L_BT + (64 + 4 * (g & 1) + q) * 128 + (((2 * rp + (g >> 1)) ^ f) << 5) + 8 * p4;
  const u32x2 b0 = trread(bb), b1 = trread(bb + 2048), b4 = trread(bb4);
  WAITLG0();
  BARRIER();
  auto mix = [&](f32x4 a, u32x2 b, u32 gq) {
    const float gg = lo16(gq), g1 = hi16(gq);
    return (u32x2){pk2(fmaf(gg, a[0], g1 * lo16(b.x)), fmaf(gg, a[1], g1 * hi16(b.x))), pk2(fmaf(gg, a[2], g1 * lo16(b.y)), fmaf(gg, a[3], g1 * hi16(b.y)))};
  };
#pragma unroll
  for (int s = 0; s < 3; ++s) {
    o0 += __builtin_bit_cast(f32x4, *(lds_u32x4*)(rw_ + hq * 6144 + s * 2048));
    o1 += __builtin_bit_cast(f32x4, *(lds_u32x4*)(rw_ + hq * 6144 + s * 2048 + 1024));
  }
  const rsrc_t ro = mkrsrc(out, S * D * 2);
  st16(swap16(mix(o0, b0, gv), mix(o1, b1, gv)), ro, c * 1152 + (g & 1) * 32 + (g >> 1) * 16, (row0 + 16 * ort) * 1152 + cg * 144 + (hq & 1) * 64);
  if (hq == 3) {
#pragma unroll
    for (int s = 0; s < 3; ++s) a4 += __builtin_bit_cast(f32x4, *(lds_u32x4*)(rw_ + 24576 + s * 1024));
    const u32x2 m4 = mix(a4, b4, gv4);
    st16(swap16(m4, m4), ro, (g & 1) ? OOB : (c + 8 * g) * 1152, (row0 + 32 * rp) * 1152 + cg * 144 + 128);
  }
  if (w == 0) STAMP(1, 4);
  if (w == 7) STAMP(1, 5);
}

extern "C" void kernel_launch(void* const* d_in, const int* in_sizes, int n_in, void* d_out, int out_size, void* d_ws,
                              size_t ws_size, hipStream_t stream) {
  char* ws = (char*)d_ws;
  (void)hipFuncSetAttribute((const void*)k1, hipFuncAttributeMaxDynamicSharedMemorySize, K1_LDS);
  (void)hipFuncSetAttribute((const void*)k2, hipFuncAttributeMaxDynamicSharedMemorySize, K2_LDS);
  k1<<<256, 512, K1_LDS, stream>>>((const u16*)d_in[0], (const u16*)d_in[1], (const u16*)d_in[2], (const u16*)d_in[4], (const u16*)d_in[5],
                                   (const u16*)d_in[6], (const u16*)d_in[7], (const u16*)d_in[8], (const u16*)d_in[9],
                                   (const u16*)d_in[10], (const u16*)d_in[11], (const u16*)d_in[12], ws);
  k2<<<256, 512, K2_LDS, stream>>>((const u16*)d_in[2], ws, (u16*)d_out);
}
```

```cpp
#include <hip/hip_runtime.h>
#include <stdint.h>
#define CODEPF 35200

typedef unsigned short u16;
typedef unsigned int u32;
typedef __attribute__((ext_vector_type(8))) __bf16 bf16x8;
typedef __attribute__((ext_vector_type(2))) __bf16 bf16x2;
typedef __attribute__((ext_vector_type(16))) float f32x16;
typedef __attribute__((ext_vector_type(4))) float f32x4;
typedef __attribute__((ext_vector_type(2))) float f32x2;
typedef __attribute__((ext_vector_type(6))) u32 u32x6;
typedef __attribute__((ext_vector_type(4))) u32 u32x4;
typedef __attribute__((ext_vector_type(2))) u32 u32x2;
typedef __attribute__((ext_vector_type(4))) short s16x4;
typedef __attribute__((address_space(3))) char lds_char;
typedef __attribute__((address_space(3))) void lds_void;
typedef __attribute__((address_space(3))) s16x4 lds_s16x4;
typedef __attribute__((address_space(3))) u32x4 lds_u32x4;
typedef __attribute__((address_space(3))) u32x2 lds_u32x2;
typedef __attribute__((address_space(3))) u32 lds_u32;
typedef __attribute__((address_space(3))) volatile u32 lds_vu32;
typedef __attribute__((address_space(3))) u16 lds_u16;
typedef __amdgpu_buffer_rsrc_t rsrc_t;

constexpr int S = 2048, D = 576;
constexpr u32 OFF_E = 0;
constexpr u32 OFF_G = 1024;
constexpr u32 OFF_KVT = OFF_G + 16384;
constexpr u32 OFF_KVP = OFF_KVT + 32768;
constexpr u32 OFF_PQ = OFF_KVP + 32 * 32768;
constexpr u32 OFF_BT = OFF_PQ + 128 * 24576;
constexpr u32 OFF_A = OFF_BT + 576 * 5504;
constexpr u32 OFF_END = OFF_A + 1024;
constexpr int OOB = 0x40000000;

__device__ __forceinline__ u32 pk2(float a, float b) {
  f32x2 v = {a, b};
  bf16x2 r = __builtin_convertvector(v, bf16x2);
  return __builtin_bit_cast(u32, r);
}
__device__ __forceinline__ float lo16(u32 v) { return __uint_as_float(v << 16); }
__device__ __forceinline__ float hi16(u32 v) { return __uint_as_float(v & 0xffff0000u); }
__device__ __forceinline__ f32x4 mfma(u32x4 a, u32x4 b, f32x4 c) {
  return __builtin_amdgcn_mfma_f32_16x16x32_bf16(__builtin_bit_cast(bf16x8, a), __builtin_bit_cast(bf16x8, b), c, 0, 0, 0);
}
__device__ __forceinline__ f32x16 mfma32(u32x4 a, u32x4 b, f32x16 c) {
  return __builtin_amdgcn_mfma_f32_32x32x16_bf16(__builtin_bit_cast(bf16x8, a), __builtin_bit_cast(bf16x8, b), c, 0, 0, 0);
}
__device__ __forceinline__ f32x4 mfma16(u32x2 a, u32x2 b, f32x4 c) {
  return __builtin_amdgcn_mfma_f32_16x16x16bf16_1k(__builtin_bit_cast(s16x4, a), __builtin_bit_cast(s16x4, b), c, 0, 0, 0);
}
__device__ __forceinline__ u32x2 trread(lds_char* p) {
  s16x4 v = __builtin_amdgcn_ds_read_tr16_b64_v4i16((lds_s16x4*)p);
  return __builtin_bit_cast(u32x2, v);
}
__device__ __forceinline__ u32x4 cat(u32x2 a, u32x2 b) { return (u32x4){a.x, a.y, b.x, b.y}; }
__device__ __forceinline__ f32x2 cmul(f32x2 a, f32x2 b) {
  f32x2 t = {a.x * b.x, a.x * b.y};
  f32x2 s = {-b.y, b.x};
  f32x2 ay = {a.y, a.y};
  return __builtin_elementwise_fma(ay, s, t);
}
__device__ __forceinline__ rsrc_t mkrsrc(const void* p, u32 bytes) {
  return __builtin_amdgcn_make_buffer_rsrc((void*)p, 0, bytes, 0x00020000);
}
__device__ __forceinline__ void dma16(rsrc_t r, lds_char* dst, int voff, int soff) {
  __builtin_amdgcn_raw_ptr_buffer_load_lds(r, (lds_void*)dst, 16, voff, soff, 0, 0);
}
#define SAUX 16
__device__ __forceinline__ void st16(u32x4 v, rsrc_t r, int voff, int soff) {
  __builtin_amdgcn_raw_buffer_store_b128(v, r, voff, soff, SAUX);
  asm volatile("s_nop 1" :: "v"(v));
}
__device__ __forceinline__ u32x4 swap16(u32x2 a, u32x2 b) {
  auto r0 = __builtin_amdgcn_permlane16_swap(a.x, b.x, false, false);
  auto r1 = __builtin_amdgcn_permlane16_swap(a.y, b.y, false, false);
  return (u32x4){r0[0], r1[0], r0[1], r1[1]};
}
__device__ __forceinline__ u32 phi2(float a0, float a1) {
  constexpr float L2E = 1.44269504088896f;
  f32x2 a = {a0, a1};
  f32x2 ar = __builtin_elementwise_fma(a, (f32x2){L2E, L2E}, (f32x2){-L2E, -L2E});
  f32x2 m = {__builtin_amdgcn_fmed3f(a0, __builtin_amdgcn_exp2f(ar.x), 1.f), __builtin_amdgcn_fmed3f(a1, __builtin_amdgcn_exp2f(ar.y), 1.f)};
  m += (f32x2){1e-6f, 1e-6f};
  return pk2(m.x, m.y);
}
#define BARRIER() asm volatile("s_barrier" ::: "memory")
#define WAITVM(n) asm volatile("s_waitcnt vmcnt(" #n ")" ::: "memory")
#define WAITLG0() do { __builtin_amdgcn_s_waitcnt(0xC07F); asm volatile("" ::: "memory"); } while (0)
#define OPQ(s) asm volatile("" : "+s"(s) :: "memory")
#define SB() __builtin_amdgcn_sched_barrier(0)

constexpr int K1_LDS = 163840;
constexpr int L_W8 = K1_LDS - 576 * 32, L_W = L_W8 - 576 * 128, L_F = 55296, L_T = L_F + 64;
#define PK1 10
#define PK2 20
#define PK3 30
template <int N> __device__ __forceinline__ void waitvm() { asm volatile("s_waitcnt vmcnt(%0)" :: "n"(N) : "memory"); }
__device__ __forceinline__ void ldsadd(lds_vu32* p, int lane) {
  if (lane == 0) asm volatile("ds_add_u32 %0, %1" :: "v"(p), "v"(1u) : "memory");
}
__device__ __forceinline__ void ldswait(lds_vu32* p, u32 n) {
#pragma unroll 1
  for (int sp = 0; sp < 100000; ++sp) {
    u32 v;
    asm volatile("ds_read_b32 %0, %1\n s_waitcnt lgkmcnt(0)" : "=v"(v) : "v"(p) : "memory");
    if (__builtin_amdgcn_readfirstlane(v) >= n) break;
    __builtin_amdgcn_s_sleep(1);
  }
}

template <bool Q>
__device__ __forceinline__ void g1_load(lds_char* sm, int lw, int lane, const u16* x, const u16* wqkv, int rb, int cj, int rot, u32 ep, char* ws,
                                        rsrc_t ro, int vw, int v8, int sw, int s8, u32x4 (&Wr)[41]) {
  constexpr int SL = Q ? 40960 : 34816;
  constexpr int NB = Q ? 6 : 5, ND = NB + 4;
  const rsrc_t rx = mkrsrc(x, S * D * 2), rw = mkrsrc(wqkv, D * 3 * D * 2);
  int va[4], vb[NB];
  {
    const int r8 = lane >> 3;
    const int ab = __umul24(rb * 128 + lw * 32 + r8, 1152);
    const int c0 = ((lane & 7) ^ (r8 >> 1)) << 4;
    va[0] = ab + c0; va[1] = ab + 8 * 1152 + (c0 ^ 64); va[2] = va[0] + 16 * 1152; va[3] = va[1] + 16 * 1152;
  }
  if (Q) {
#pragma unroll
    for (int j = 0; j < 3; ++j) {
      int idx = j * 64 + lane;
      int rho = __umul24(idx, 2731) >> 16, pos = idx - __umul24(rho, 24);
      int b0 = __umul24(lw * 16 + rho, 3456) + cj * 384, sw = (pos ^ (rho & 2)) << 4;
      vb[j] = b0 + sw;
      vb[j + 3] = b0 + 8 * 3456 + (sw ^ 64);
    }
  } else {
    const int u = ((lane >> 1) & 7) ^ (lane >> 4);
    const int b0 = __umul24(lw * 16 + (lane >> 4), 3456) + 1152 + cj * 144 + (u & 2) * 576 + ((lane & 1) << 4) + ((u & 1) << 5);
    vb[0] = b0 + ((u & 4) << 4); vb[1] = vb[0] + 4 * 3456;
    vb[2] = b0 + 8 * 3456 + ((~u & 4) << 4); vb[3] = vb[2] + 4 * 3456;
    const int rho = (lane >> 1) & 15;
    const int kr = (rho & 3) | ((rho & 4) << 1) | ((rho & 8) >> 1);
    vb[4] = __umul24(lw * 16 + kr, 3456) + 1152 + 128 + cj * 144 + (lane & 1) * 1152;
  }
  auto ldo = [&](int j0, int j1) {
#pragma unroll
    for (int j = j0; j < j1; ++j)
      Wr[j] = j < 18 || j >= 23 ? __builtin_amdgcn_raw_buffer_load_b128(ro, vw + (j < 18 ? 0 : 128), sw + (j < 18 ? j : j - 23) * 8 * 1152, 0)
                                : __builtin_amdgcn_raw_buffer_load_b128(ro, j < 22 || lane < 32 ? v8 : OOB, s8 + (j - 18) * 32 * 1152, 0);
  };
  int st = rot;
  auto stage = [&](int sl) {
    lds_char* base = sm + sl * SL;
#pragma unroll
    for (int j = 0; j < 4; ++j) dma16(rx, base + (lw * 4 + j) * 1024, va[j], st * 128);
#pragma unroll
    for (int j = 0; j < NB; ++j) {
      if (Q) dma16(rw, base + 16384 + (lw * 6 + j) * 1024, vb[j], st * 64 * 3456);
      else if (j < 4) dma16(rw, base + 16384 + (lw * 4 + j) * 1024, vb[j], st * 64 * 3456);
      else if (lane < 32) dma16(rw, base + 32768 + lw * 512, vb[j], st * 64 * 3456);
    }
    st = st == 8 ? 0 : st + 1;
  };
#pragma unroll 1
  for (int it = 0; it < 9; ++it) {
    stage(it & 3);
    if (it == 0 || it == 3) continue;
    if (it < 3) waitvm<ND>(); else waitvm<2 * ND>();
    BARRIER();
  }
  asm volatile("" :: "s"(ep));
  if (lane < 8) asm volatile("ds_write_b32 %0, %1" :: "v"(sm + L_F + lane * 4), "v"(0u) : "memory");
  ldo(0, PK1);
  waitvm<ND + PK1>();
  BARRIER();
  ldo(PK1, PK2);
  waitvm<PK2>();
  WAITLG0();
  BARRIER();
  ldo(PK2, PK3);
  BARRIER();
  ldo(PK3, 41);
}

template <bool Q>
__device__ __forceinline__ void g1_main(lds_char* sm, int w, int lane, f32x4 (&acc)[4][Q ? 6 : 5], u32 ep, char* ws) {
  constexpr int SL = Q ? 40960 : 34816;
  constexpr int NT = Q ? 6 : 5, NS = 2 * NT;
  constexpr int RS = Q ? 384 : 256;
  const int wr = w >> 1, wc = w & 1;
  const int r = lane & 15, g = lane >> 4, q = (lane >> 2) & 3, p = lane & 3;
  const int sw = (r >> 1) & 7;
  lds_char* ao[2][2];
  lds_char* bo[6];
#pragma unroll
  for (int kk = 0; kk < 2; ++kk) {
    int a = (64 * wr + r) * 128 + (((4 * kk + g) ^ sw) << 4);
    ao[kk][0] = sm + (Q ? a : a + 4096 * wc);
    ao[kk][1] = sm + (Q ? a + 4096 : a + 4096 * (1 - wc));
  }
  if (Q) {
    const int f = (q >> 1) | ((g & 1) << 1);
#pragma unroll
    for (int t = 0; t < 6; ++t) bo[t] = sm + (16384 + (8 * g + q) * 384 + ((wc * 192 + 96 * (t / 3) + 8 * (t % 3) + 24 * p) ^ (f << 5)));
  } else {
    const int f = q | ((g & 1) << 2);
#pragma unroll
    for (int t = 0; t < 4; ++t) bo[t] = sm + (16384 + (8 * g + q) * 256 + 8 * p + (((4 * wc + t) ^ f) << 5));
    bo[4] = sm + (32768 + (16 * (g >> 1) + 4 * (g & 1) + q) * 32 + 8 * p);
  }
  u32x4 A[2][4];
  u32x2 B[NT][2];
  auto rdA = [&](int kk, int i) {
    A[kk][i] = *(lds_u32x4*)(Q ? ao[kk][0] + 2048 * i : ao[kk][i >> 1] + 2048 * (i & 1));
  };
  auto rdB = [&](int u, int h) {
    const int kk = u / NT, t = u % NT;
    if (!Q && t == 4) B[t][h] = trread(bo[4] + kk * 1024 + h * 256);
    else B[t][h] = trread(bo[t] + kk * 32 * RS + h * 4 * RS);
  };
#pragma unroll
  for (int i = 0; i < 4; ++i)
#pragma unroll
    for (int t = 0; t < NT; ++t)
      if (Q || t < 4 || i < 2) asm volatile("" : "+v"(acc[i][t]));
  asm volatile("" : "+v"(ao[0][0]), "+v"(ao[1][0]), "+v"(bo[0]), "+v"(bo[1]), "+v"(bo[2]), "+v"(bo[3]));
  if (!Q) asm volatile("" : "+v"(ao[0][1]), "+v"(ao[1][1]), "+v"(bo[4]));
  else asm volatile("" : "+v"(bo[4]), "+v"(bo[5]));
  BARRIER();
  rdB(0, 0); rdB(0, 1); SB();
  rdA(0, 0); rdA(0, 1); rdA(0, 2); rdA(0, 3); SB();
#pragma unroll
  for (int u = 1; u < NT - 1; ++u) { rdB(u, 0); rdB(u, 1); SB(); }
  asm volatile("" :: "s"(ep));
  constexpr int MB = Q ? 31 : 25;
#pragma unroll 1
  for (int st = 0; st < 9; ++st) {
    const int dl = (st & 3) == 3 ? -3 * SL : SL;
    auto adv = [&](lds_char*& a) { asm volatile("v_add_u32 %0, %1, %0" : "+v"(a) : "s"(dl)); };
    __builtin_amdgcn_s_waitcnt(0xC27F);
#pragma unroll
    for (int u = 0; u < NS; ++u) {
      const int kk = u / NT, t = u % NT;
#pragma unroll
      for (int i = 0; i < 4; ++i) {
        if (!Q && t == 4 && i >= 2) continue;
        const int m = Q ? 4 * u + i : 18 * kk + 4 * t + i;
        const u32x4 Bf = cat(B[t][0], B[t][1]);
        acc[i][t] = Q ? mfma(Bf, A[kk][i], acc[i][t]) : mfma(A[kk][i], Bf, acc[i][t]);
        SB();
        if (m < 2) rdB(NT - 1, m);
        else if (m == 2 || m == 3) rdA(1, m - 2);
        else if (m == 6 || m == 7) rdA(1, m - 4);
        else if (u >= 1 && u <= NT && i < 2) rdB(u + NT - 1, i);
        else if (m == MB - 5) { adv(ao[0][0]); adv(Q ? ao[1][0] : ao[0][1]); }
        else if (m == MB - 4) { adv(bo[0]); adv(bo[1]); }
        else if (m == MB - 3) { adv(bo[2]); adv(bo[3]); }
        else if (m == MB - 2) { if (Q) { adv(bo[4]); adv(bo[5]); } else { adv(ao[1][0]); adv(ao[1][1]); } }
        else if (m == MB - 1 && !Q) { adv(bo[4]); }
        else if (m == MB) {
          WAITLG0();
          BARRIER();
        }
        else if (m == MB + 1) { rdB(0, 0); rdB(0, 1); }
        else if (m == MB + 2) { rdA(0, 0); rdA(0, 1); }
        else if (m == MB + 3) { rdA(0, 2); rdA(0, 3); }
        else if (m == MB + 4) { rdB(1, 0); rdB(1, 1); }
        else if (u >= NT + 3 && i < 2) rdB(u - NT - 1, i);
        SB();
      }
    }
  }
}

constexpr int TROW = 544;
constexpr int TBW = 13632;
constexpr int UOFF = 8 * TBW;
constexpr int UW = 4608;
constexpr int MOFF = UOFF + 8 * UW;
constexpr int XG = 2320, XW = 9280;
constexpr int SPIN = 4000;
typedef __attribute__((address_space(4))) const u32 cu32;
__device__ __forceinline__ u32 mktag(u32 e) { return ((e & 0x3fffu) | ((e << 1) & 0x3fff8000u) | ((e << 2) & 0x80000000u)) ^ 0xA5C3965Bu & 0xBFFFBFFFu; }
#define LAUX 16
__device__ __forceinline__ u32x4 ld16(rsrc_t r, int voff, int soff) { return __builtin_amdgcn_raw_buffer_load_b128(r, voff, soff, LAUX); }
#define PT 0
#define PTN 8
#define NOSAMP 0
#define EARLYP 0
#define GRED 0
#define PWAIT 1
#define KMISS 1
#define PMID 0
#define PAUX LAUX
#define KAUX LAUX
#define TCH 0
#define TSTR 128
#define TSPAN 16384
#define TAUX 16
#define TOUT 1
#define RDLY 0
#define SPOS 0
#define ACK 1
#define IW 0
#define WLG 1
#define KVS 0
#define AH 1
#define EHOIST 1
#define EPRIO 2
#define EILP 3
#define EILP0 6
#define QDLY 28
#define SSLEEP 2
#define ROTX (slot < 16 ? (slot * 5) % 9 : xcd)
#define QPF 0
#define QAUX 16
#define KVW 1
#define PFPOS 1
#define KWP 0
#define RSLEEP 2
#define OAUX 16
__device__ __forceinline__ void STO(u32x4 v, rsrc_t r, int voff, int soff) {
  __builtin_amdgcn_raw_buffer_store_b128(v, r, voff, soff, OAUX);
  asm volatile("s_nop 1" :: "v"(v));
}
__device__ __forceinline__ bool allok(u32 bad, bool act) { return __builtin_amdgcn_ballot_w64(bad != 0 && act) == 0; }
__device__ __forceinline__ float xadd16(float v) {
  auto r = __builtin_amdgcn_permlane16_swap(__float_as_uint(v), __float_as_uint(v), false, false);
  return __uint_as_float(r[0]) + __uint_as_float(r[1]);
}
__device__ __forceinline__ float xadd32(float v) {
  auto r = __builtin_amdgcn_permlane32_swap(__float_as_uint(v), __float_as_uint(v), false, false);
  return __uint_as_float(r[0]) + __uint_as_float(r[1]);
}

__device__ __forceinline__ void ldw(lds_char* sm, lds_vu32* cnt, int w, int lane, u32x4 (&Wf)[18]) {
  const int g = lane >> 4, q = (lane >> 2) & 3, p4 = lane & 3, u = w & 3;
  lds_char* b0 = sm + L_W + (4 * g + q) * 128 + 8 * p4;
  lds_char* bh[4];
#pragma unroll
  for (int k = 0; k < 4; ++k) bh[k] = b0 + ((u ^ ((k + 2 * g + (q >> 1)) & 3)) << 5);
  lds_char* bx = sm + L_W + (72 * (q >> 1) + 18 * g + 16 + (q & 1)) * 128 + 8 * p4 + ((u ^ g) << 5);
  ldswait(cnt, 4u);
#pragma unroll
  for (int s = 0; s < 16; ++s) Wf[s] = cat(trread(bh[(2 * s) & 3] + 2 * s * 2304), trread(bh[(2 * s + 1) & 3] + (2 * s + 1) * 2304));
#pragma unroll
  for (int s = 0; s < 2; ++s) Wf[16 + s] = cat(trread(bx + s * 36864), trread(bx + s * 36864 + 18432));
#pragma unroll
  for (int s = 0; s < 18; ++s) asm volatile("" : "+v"(Wf[s]));
}

extern "C" __global__ __launch_bounds__(512, 1) void k1(const u16* __restrict__ x, const u16* __restrict__ wqkv, const u16* __restrict__ wo,
                                             const u16* __restrict__ Are, const u16* __restrict__ Aim,
                                             const u16* __restrict__ Bre, const u16* __restrict__ Bim,
                                             const u16* __restrict__ Cre, const u16* __restrict__ Cim,
                                             const u16* __restrict__ logdt, const u16* __restrict__ dskip,
                                             const u16* __restrict__ gw, char* __restrict__ ws, u16* __restrict__ out) {
  extern __shared__ __attribute__((aligned(16))) char shm_[];
  lds_char* sm = (lds_char*)shm_;
  const int tid = threadIdx.x, lane = tid & 63;
  const int w = __builtin_amdgcn_readfirstlane(tid >> 6);
  const int xcd = blockIdx.x & 7, slot = blockIdx.x >> 3;
  const int c = lane & 15, g = lane >> 4;
  const f32x4 z4 = {0.f, 0.f, 0.f, 0.f};
  const u32 ep = *(cu32*)(ws + OFF_E);
  const rsrc_t rk = mkrsrc(ws, OFF_END);
  if (__builtin_expect(slot < 22, 1)) {
    const int j2 = xcd * 22 + slot, cg = j2 & 3;
    const rsrc_t rw = mkrsrc(wo, D * D * 2);
    const int vw = (lane >> 3) * 1152 + (((lane & 7) ^ ((lane >> 3) & 6)) << 4);
    lds_vu32* flag = (lds_vu32*)(sm + L_F);
    u32x4 Wf[18];
    const int rg = j2 >> 2;
    const int d2 = rg == 42 ? 1 : 2;
    const bool act = lane < 48;
    int sp_, dp2, vp;
    auto setp = [&]() {
      sp_ = OFF_PQ + 3 * rg * 24576 + w * 3072; dp2 = __builtin_amdgcn_readfirstlane(d2 * 24576); vp = act ? lane * 16 : OOB;
      asm volatile("" : "+s"(sp_), "+s"(dp2), "+v"(vp));
    };
    const int w3 = w < 3 ? w : 0;
    int rk3[3];
#pragma unroll
    for (int k = 0; k < 3; ++k) rk3[k] = k + w3 < 3 ? k + w3 : k + w3 - 3;
    int vk, vx, sk, sb_, s8_, vb, v8, vg, sg_[3], vo0, vo1, so;
    lds_char *ob[3], *ow, *ox, *w8, *x8, *tw, *tw8, *tr[4];
    auto addrs = [&]() {
      const int q = (lane >> 2) & 3, p4 = lane & 3;
      setp();
      vk = act ? g * 320 + c * 16 : OOB;
      vx = act ? q * 1024 + g * 320 + 256 + ((c + 2) & 3) * 16 : OOB;
      sk = OFF_KVT + w * 4096;
      sb_ = OFF_BT + (cg * 128 + 16 * w) * 5504 + rg * 128, s8_ = OFF_BT + (512 + cg * 16) * 5504 + rg * 128 + w * 32;
      vb = __umul24(lane >> 3, 5504) + (lane & 7) * 16, v8 = w < 3 ? __umul24(lane >> 2, 5504) + p4 * 16 : OOB, vg = c * 8;
      int uo[4];
#pragma unroll
      for (int k = 0; k < 3; ++k) {
        sg_[k] = OFF_G + rg * 384 + (rk3[k] < d2 ? rk3[k] : d2) * 128;
        uo[k] = rk3[k] <= d2 ? rk3[k] * 16 * 1152 + w * 32 : OOB;
        ob[k] = sm + lane * 16 + rk3[k] * 18432;
      }
      uo[3] = w <= d2 ? w * 16 * 1152 + 1024 - cg * 224 : OOB;
      vo0 = c * 1152 + (g >> 1) * 16 + ((g & 1) ? uo[2] : uo[1]), vo1 = c * 1152 + (g >> 1) * 16 + ((g & 1) ? uo[3] : uo[0]);
      so = rg * 48 * 1152 + cg * 256;
      ow = sm + w * 2048 + lane * 16;
      ox = sm + (16 + (w >> 2)) * 1024 + lane * 16 + (w & 3) * 4;
      w8 = sm + L_W8 + (4 * g + q) * 32 + 8 * p4;
      x8 = sm + L_W8 + (72 * (q >> 1) + 18 * g + 16 + (q & 1)) * 32 + 8 * p4;
      tw = sm + L_T + w * 1536 + lane * 12;
      tw8 = sm + L_T + 12288 + w3 * 768 + lane * 12;
#pragma unroll
      for (int k = 0; k < 3; ++k) tr[k] = sm + L_T + w * 1536 + (4 * g + q) * 96 + 32 * rk3[k] + 8 * p4;
      tr[3] = sm + L_T + 12288 + w3 * 776 + (4 * g + q) * 48 + 8 * p4;
      asm volatile("" : "+v"(vk), "+v"(vx), "+v"(vb), "+v"(v8), "+v"(vg), "+v"(vo0), "+v"(vo1), "+v"(ow), "+v"(ox), "+v"(w8), "+v"(x8));
      asm volatile("" : "+v"(ob[0]), "+v"(ob[1]), "+v"(ob[2]), "+v"(tw), "+v"(tw8), "+v"(tr[0]), "+v"(tr[1]), "+v"(tr[2]), "+v"(tr[3]));
      asm volatile("" : "+s"(sk), "+s"(sb_), "+s"(s8_), "+s"(sg_[0]), "+s"(sg_[1]), "+s"(sg_[2]), "+s"(so));
    };
    u32x4 P[3][4];
    auto ldp = [&]() {
#pragma unroll
      for (int rt = 0; rt < 3; ++rt)
#pragma unroll
        for (int h = 0; h < 4; ++h) P[rt][h] = __builtin_amdgcn_raw_buffer_load_b128(rk, vp + h * 768, sp_ + (rt < 2 ? rt * 24576 : dp2), PAUX);
    };
    if (__builtin_expect(w < 4, 1)) {
      u32x4 Wr[41];
      const int so = w * 144 * 1152 + cg * 256, v8 = (lane >> 1) * 1152 + (lane & 1) * 16;
      if (slot < 16) g1_load<false>(sm, w, lane, x, wqkv, (xcd >> 1) * 4 + (slot >> 2), (xcd & 1) * 4 + (slot & 3), ROTX, ep, ws, rw, vw, v8, so, so + 1024 - cg * 224, Wr);
      else g1_load<true>(sm, w, lane, x, wqkv, 2 * xcd + (slot - 16) / 3, (slot - 16) % 3, ROTX, ep, ws, rw, vw, v8, so, so + 1024 - cg * 224, Wr);
      if (w == 0) __builtin_amdgcn_raw_buffer_store_b32(mktag(ep), rk, lane == 0 ? blockIdx.x * 4 : OOB, OFF_A, 16);
      lds_char* wd = sm + L_W + w * 18432 + lane * 16;
      lds_char* wd8 = sm + L_W8 + w * 4608 + lane * 16;
      if (j2 >= 172) __builtin_amdgcn_s_waitcnt(0x0F70);
      if (j2 >= 172) return;
      addrs();
      SB();
#pragma unroll
      for (int j = 0; j < KVS; ++j) *(lds_u32x4*)(wd + j * 1024) = Wr[j];
      if (slot < 16) ldswait(flag + 4, 4u);
#pragma unroll
      for (int j = KVS; j < 18; ++j) *(lds_u32x4*)(wd + j * 1024) = Wr[j];
#pragma unroll
      for (int j = 0; j < 5; ++j)
        if (j < 4 || lane < 32) *(lds_u32x4*)(wd8 + j * 1024) = Wr[18 + j];
      WAITLG0();
      ldsadd(flag, lane);
      ldw(sm, flag, w, lane, Wf);
      WAITLG0();
      ldsadd(flag + 1, lane);
      ldswait(flag + 1, 4u);
#pragma unroll
      for (int j = 0; j < 18; ++j) {
        *(lds_u32x4*)(wd + j * 1024) = Wr[23 + j];
      }
      WAITLG0();
      ldsadd(flag + 2, lane);
    } else if (slot < 16) {
      const int rb = (xcd >> 1) * 4 + (slot >> 2), kvj = (xcd & 1) * 4 + (slot & 3);
      const int cw = w - 4, wr = cw >> 1, wc = cw & 1;
      const int e = lane & 31, hi = lane >> 5;
      const int pass = 1;
      {
      f32x4 acc[4][5];
      const float i9 = c < 8 ? 1.f : 0.f;
#pragma unroll
      for (int i = 0; i < 4; ++i) {
        acc[i][0] = acc[i][1] = (f32x4){1.f, 1.f, 1.f, 1.f};
        acc[i][2] = acc[i][3] = z4;
        acc[i][4] = (f32x4){i9, i9, i9, i9};
      }
      lds_char* pa = sm + hi * XG + (18 * cw + e - (e >> 2)) * 16;
      lds_char* pb = sm + hi * XG + (e < 18 ? 72 + 18 * cw + e : 144) * 16;
      int vo = e < 20 ? (e >> 2) * 2048 + (e & 3) * 32 + hi * 16 : OOB;
      lds_char* x0 = sm + (2 * wr + wc) * XW + g * XG + c * 16 + wc * 512;
      lds_char* x1 = sm + (2 * wr + 1 - wc) * XW + g * XG + c * 16 + wc * 512;
      lds_char* x2 = sm + (2 * wr + wc) * XW + g * XG + c * 16 + (c < 8 ? 1024 : 2048);
      lds_char* x3 = sm + cw * XW + lane * XG + 2304;
      if (!pass) {
        x0 = x1 = x2 = x3 = sm + 4 * 34816 + lane * 16;
        vo = OOB;
      }
      asm volatile("" : "+v"(pa), "+v"(pb), "+v"(vo), "+v"(x0), "+v"(x1), "+v"(x2), "+v"(x3));
      if (pass) {
      g1_main<false>(sm, cw, lane, acc, ep, ws);
      __builtin_amdgcn_s_setprio(EPRIO);
      }
      {
        SB();
        constexpr float L2E = 1.44269504088896f;
        f32x2 ar[20], md[20];
        u32 pk[20], pv[20];
#define KP(p, vo_) ((f32x2){acc[((p) >> 2 < 4 ? ((p) >> 3) * 2 : 0) + (((p) >> 1) & 1)][(p) >> 2 < 4 ? (((p) >> 2) & 1) + (vo_) : 4][((p) & 1) * 2], \
                           acc[((p) >> 2 < 4 ? ((p) >> 3) * 2 : 0) + (((p) >> 1) & 1)][(p) >> 2 < 4 ? (((p) >> 2) & 1) + (vo_) : 4][((p) & 1) * 2 + 1]})
#pragma unroll
        for (int p = 0; p < 2; ++p) ar[p] = __builtin_elementwise_fma(KP(p, 0), (f32x2){L2E, L2E}, (f32x2){-L2E, -L2E});
        SB();
#pragma unroll
        for (int p = 0; p < 23; ++p) {
          if (p < 20) ar[p].x = __builtin_amdgcn_exp2f(ar[p].x);
          if (p + 2 < 20) ar[p + 2] = __builtin_elementwise_fma(KP(p + 2, 0), (f32x2){L2E, L2E}, (f32x2){-L2E, -L2E});
          if (p >= 1 && p < 21) md[p - 1].x = __builtin_amdgcn_fmed3f(KP(p - 1, 0).x, ar[p - 1].x, 1.f);
          if (p < 20) pv[p] = pk2(KP(p, 2).x, KP(p, 2).y);
          SB();
          if (p < 20) ar[p].y = __builtin_amdgcn_exp2f(ar[p].y);
          if (p >= 1 && p < 21) md[p - 1].y = __builtin_amdgcn_fmed3f(KP(p - 1, 0).y, ar[p - 1].y, 1.f);
          if (p >= 2 && p < 22) md[p - 2] += (f32x2){1e-6f, 1e-6f};
          if (p >= 3) pk[p - 3] = pk2(md[p - 3].x, md[p - 3].y);
          if ((p & 3) == 3 && p < 16) *(lds_u32x4*)((p >> 3 ? x1 : x0) + 1152 + ((p >> 2) & 1) * 256) = (u32x4){pv[p - 3], pv[p - 2], pv[p - 1], pv[p]};
          if ((p & 3) == 2 && p >= 6 && p < 22) *(lds_u32x4*)(((p - 6) >> 3 ? x1 : x0) + (((p - 6) >> 2) & 1) * 256) = (u32x4){pk[p - 6], pk[p - 5], pk[p - 4], pk[p - 3]};
          SB();
        }
#undef KP
        *(lds_u32x4*)x2 = c < 8 ? (u32x4){pk[16], pk[17], pk[18], pk[19]} : (u32x4){pv[16], pv[17], pv[18], pv[19]};
        SB();
        if (lane < 4) *(lds_u32x4*)x3 = (u32x4){0x3f803f80u, 0x3f803f80u, 0x3f803f80u, 0x3f803f80u};
      }
      u32 tag = 0;
      int so = OFF_KVP + (kvj * 4 + cw) * 32768 + rb * 128;
      if (pass) {
      tag = mktag(ep);
      asm volatile("" : "+s"(tag), "+s"(so));
      WAITLG0();
      ldsadd(flag + 5, lane);
      ldswait(flag + 5, 4u);
      }
      u32x4 fa[8], fb[8];
#pragma unroll
      for (int m = 0; m < 8; ++m) {
        fa[m] = *(lds_u32x4*)(pa + (m >> 1) * XW + (m & 1) * 2 * XG);
        fb[m] = *(lds_u32x4*)(pb + (m >> 1) * XW + (m & 1) * 2 * XG);
      }
      if (pass) ldsadd(flag + 4, lane);
      f32x16 kv = {0.f, 0.f, 0.f, 0.f, 0.f, 0.f, 0.f, 0.f, 0.f, 0.f, 0.f, 0.f, 0.f, 0.f, 0.f, 0.f};
#pragma unroll
      for (int m = 0; m < 8; ++m) kv = mfma32(fa[m], fb[m], kv);
#pragma unroll
      for (int k = 0; k < 3; ++k)
        st16((u32x4){__float_as_uint(kv[4 * k]), __float_as_uint(kv[4 * k + 1]), __float_as_uint(kv[4 * k + 2]), tag}, rk, vo, so + k * 10240);
      __builtin_amdgcn_s_setprio(0);
      }
      addrs();
    } else {
      const int rb = 2 * xcd + (slot - 16) / 3, qj = (slot - 16) % 3;
      const int cw = w - 4, wr = cw >> 1, wc = cw & 1;
      f32x4 acc[4][6];
#pragma unroll
      for (int i = 0; i < 4; ++i)
#pragma unroll
        for (int t = 0; t < 6; ++t) acc[i][t] = (f32x4){1.f, 1.f, 1.f, 1.f};
      g1_main<true>(sm, cw, lane, acc, ep, ws);
      const u32 tag = mktag(ep);
      const int so = OFF_PQ + (rb * 8 + wr * 4) * 24576 + (32 * qj + 16 * wc) * 256;
      const int vo = g * 512 + c * 16;
#pragma unroll
      for (int i = 0; i < 4; ++i)
#pragma unroll
        for (int u = 0; u < 2; ++u) {
          f32x4 a = acc[i][3 * u], b = acc[i][3 * u + 1], d = acc[i][3 * u + 2];
          st16((u32x4){phi2(a[0], a[1]), phi2(a[2], a[3]), phi2(b[0], b[1]), tag}, rk, vo + u * 2048, so + i * 24576);
          st16((u32x4){phi2(b[2], b[3]), phi2(d[0], d[1]), phi2(d[2], d[3]), tag}, rk, vo + u * 2048 + 256, so + i * 24576);
        }
      if (j2 >= 172) return;
      addrs();
    }
    {
      const u32 tag = mktag(ep);
      const int q = (lane >> 2) & 3, p4 = lane & 3;
      const int id = xcd * 16 + slot, rho = (id & 3) * 4 + w - 4;
      const bool red = !GRED && slot < 16 && w >= 4 && rho < 15;
#define RFL(x) x = __builtin_amdgcn_readfirstlane(x)
      RFL(sk); RFL(sb_); RFL(s8_); RFL(sg_[0]); RFL(sg_[1]); RFL(sg_[2]); RFL(so); RFL(sp_); RFL(dp2);
#undef RFL
      if (PWAIT && (!EARLYP || w >= 4)) WAITVM(0);
      const int vs = lane < 12 ? (lane < 8 ? q : d2) * 24576 + p4 * 768 + 764 : OOB;
      bool pok = false, pin = false;
      if (red) {
        int sr = OFF_KVP + (id >> 2) * 32768 + rho * 2048;
        u32x4 r0, r1;
        bool kok = false;
#pragma unroll 1
        for (int sp = 0; sp < SPIN; ++sp) {
          OPQ(sr);
          r0 = ld16(rk, lane * 16, sr); r1 = ld16(rk, lane * 16 + 1024, sr);
          kok = allok((r0.w ^ tag) | (r1.w ^ tag), true);
          if (kok || (pok && PMID < 3)) break;
          __builtin_amdgcn_s_sleep(RSLEEP);
        }
        float v[3];
#pragma unroll
        for (int i = 0; i < 3; ++i) {
          float s = __uint_as_float(r0[i]) + __uint_as_float(r1[i]);
          s += __uint_as_float(__builtin_amdgcn_update_dpp(0u, __float_as_uint(s), 0x128, 0xf, 0xf, true));
          v[i] = xadd32(xadd16(s));
        }
        const float b0 = __uint_as_float(__builtin_amdgcn_update_dpp(0u, __float_as_uint(v[0]), 0xF5, 0xf, 0xf, true));
        const u32 b12 = __builtin_amdgcn_update_dpp(0u, pk2(v[1], v[2]), 0xF5, 0xf, 0xf, true);
        st16((u32x4){pk2(v[0], v[1]), pk2(v[2], b0), b12, tag}, rk, (lane & 57) == 0 ? lane * 8 : OOB, OFF_KVT + (id >> 2) * 1024 + rho * 64);
      }
      if (w >= 4) ldsadd(flag + 3, lane);
      if ((EARLYP == 0 || w >= 4) && !pok && !(NOSAMP && red)) {
        if (w >= 4 && !red) __builtin_amdgcn_s_sleep(QDLY);
#pragma unroll 1
        for (int sp = 0; sp < SPIN; ++sp) {
          OPQ(sp_);
          const u32 t = __builtin_amdgcn_raw_buffer_load_b32(rk, vs, sp_, LAUX);
          const bool ok = allok(t ^ tag, lane < 12);
          if (ok) break;
          __builtin_amdgcn_s_sleep(SSLEEP);
        }
      }
      u32x4 KE[4], KX;
      auto ldk = [&]() {
        KX = __builtin_amdgcn_raw_buffer_load_b128(rk, vx, sk, KAUX);
#pragma unroll
        for (int h = 0; h < 4; ++h) KE[h] = __builtin_amdgcn_raw_buffer_load_b128(rk, vk + h * 1024, sk, KAUX);
      };
      if ((!EARLYP || w >= 4) && !pin) ldp();
      if (w >= 4) {
        ldw(sm, flag + 2, w, lane, Wf);
      }
#pragma unroll 1
      for (int sp = 0; sp < SPIN; ++sp) {
        u32 bad = 0;
#pragma unroll
        for (int rt = 0; rt < 3; ++rt)
#pragma unroll
          for (int h = 0; h < 4; ++h) bad |= P[rt][h].w ^ tag;
        const bool ok = allok(bad, act);
        if (ok) break;
        OPQ(sp_);
#pragma unroll
        for (int rt = 0; rt < 3; ++rt)
#pragma unroll
          for (int h = 0; h < 4; ++h)
            if (act && P[rt][h].w != tag) P[rt][h] = ld16(rk, vp + h * 768, sp_ + (rt < 2 ? rt * 24576 : dp2));
      }
#pragma unroll
      for (int rt = 0; rt < 3; ++rt)
#pragma unroll
        for (int h = 0; h < 4; ++h) { P[rt][h].w = 0u; asm volatile("" : "+v"(P[rt][h])); }
      ldswait(flag + 3, 4u);
      if (KWP == 0 || (KWP == 2 && !pin)) ldk();
#pragma unroll 1
      for (int sp = 0; sp < SPIN; ++sp) {
        u32 bad = KX.w ^ tag;
#pragma unroll
        for (int h = 0; h < 4; ++h) bad |= KE[h].w ^ tag;
        if (allok(bad, act)) break;
        __builtin_amdgcn_s_sleep(1);
        OPQ(sk);
        if (act && KX.w != tag) KX = ld16(rk, vx, sk);
#pragma unroll
        for (int h = 0; h < 4; ++h)
          if (act && KE[h].w != tag) KE[h] = ld16(rk, vk + h * 1024, sk);
      }
      {
        const f32x4 eps4 = {1e-6f, 0.f, 0.f, 0.f};
        u32x4 KM[4];
        f32x4 X[3] = {eps4, eps4, eps4}, E[3][4];
        float sc[3][4];
        auto fin1 = [&](int rt) {
          const float r = __builtin_amdgcn_rcpf(X[rt][0]);
          *(lds_u32*)(ox + rt * 18432) = pk2(X[rt][2] * r, X[rt][3] * r);
          auto s32 = __builtin_amdgcn_permlane32_swap(__float_as_uint(r), __float_as_uint(r), false, false);
          auto sa = __builtin_amdgcn_permlane16_swap(s32[0], s32[0], false, false);
          auto sb = __builtin_amdgcn_permlane16_swap(s32[1], s32[1], false, false);
          sc[rt][0] = __uint_as_float(sa[0]); sc[rt][1] = __uint_as_float(sa[1]); sc[rt][2] = __uint_as_float(sb[0]); sc[rt][3] = __uint_as_float(sb[1]);
        };
        auto fin2 = [&](int rt, int hh) {
          const f32x4 a = E[rt][2 * hh] * sc[rt][2 * hh], b = E[rt][2 * hh + 1] * sc[rt][2 * hh + 1];
          *(lds_u32x4*)(ow + rt * 18432 + hh * 1024) = (u32x4){pk2(a[0], a[1]), pk2(a[2], a[3]), pk2(b[0], b[1]), pk2(b[2], b[3])};
        };
        SB();
#pragma unroll
        for (int h = 0; h < 4; ++h) {
          E[0][h] = mfma(KE[h], P[0][h], z4);
          const bool m = q == h;
          KM[h] = (u32x4){m ? KX.x : 0u, m ? KX.y : 0u, m ? KX.z : 0u, KX.w};
          SB();
        }
#pragma unroll
        for (int h = 0; h < 4; ++h)
#pragma unroll
          for (int rt = 0; rt < 3; ++rt) { X[rt] = mfma(KM[h], P[rt][h], X[rt]); SB(); }
        E[1][0] = mfma(KE[0], P[1][0], z4); SB();
        E[1][1] = mfma(KE[1], P[1][1], z4); fin1(0); SB();
        E[1][2] = mfma(KE[2], P[1][2], z4); fin1(1); SB();
        E[1][3] = mfma(KE[3], P[1][3], z4); fin1(2); SB();
        E[2][0] = mfma(KE[0], P[2][0], z4); fin2(0, 0); SB();
        E[2][1] = mfma(KE[1], P[2][1], z4); fin2(0, 1); SB();
        E[2][2] = mfma(KE[2], P[2][2], z4); fin2(1, 0); SB();
        E[2][3] = mfma(KE[3], P[2][3], z4); fin2(1, 1); SB();
        fin2(2, 0); fin2(2, 1);
        SB();
      }
      u32x4 bt[3];
      u32x2 G[3];
      auto ldbt = [&]() {
#pragma unroll
        for (int k = 0; k < 3; ++k) G[k] = __builtin_amdgcn_raw_buffer_load_b64(rk, vg, sg_[k], 16);
        bt[0] = ld16(rk, vb, sb_); bt[1] = ld16(rk, vb, sb_ + 8 * 5504); bt[2] = ld16(rk, v8, s8_); };
      ldbt();
      SB();
      WAITLG0();
      f32x4 acc[4] = {z4, z4, z4, z4};
      u32x4 F[4][3];
      if (w < 3) {
        u32x2 V[4][2];
        auto rdv = [&](int s, int h) { V[s & 3][h] = s < 16 ? trread(w8 + s * 1152 + h * 576) : trread(x8 + (s - 16) * 9216 + h * 4608); };
#pragma unroll
        for (int s = 0; s < 3; ++s) { rdv(s, 0); rdv(s, 1); }
        BARRIER();
#pragma unroll
        for (int s = 0; s < 3; ++s)
#pragma unroll
          for (int k = 0; k < 3; ++k) { F[s][k] = *(lds_u32x4*)(ob[k] + s * 1024); SB(); }
#pragma unroll
        for (int s = 0; s < 18; ++s) {
#pragma unroll
          for (int k = 0; k < 3; ++k) {
            acc[k] = mfma(Wf[s], F[s & 3][k], acc[k]);
            SB();
            if (s < 15) F[(s + 3) & 3][k] = *(lds_u32x4*)(ob[k] + (s + 3) * 1024);
            SB();
          }
          acc[3] = mfma(cat(V[s & 3][0], V[s & 3][1]), F[s & 3][0], acc[3]);
          SB();
          if (s < 15) { rdv(s + 3, 0); rdv(s + 3, 1); }
          SB();
        }
      } else {
        BARRIER();
#pragma unroll
        for (int s = 0; s < 3; ++s)
#pragma unroll
          for (int k = 0; k < 3; ++k) { F[s][k] = *(lds_u32x4*)(ob[0] + k * 18432 + s * 1024); SB(); }
#pragma unroll
        for (int s = 0; s < 18; ++s)
#pragma unroll
          for (int k = 0; k < 3; ++k) {
            acc[k] = mfma(Wf[s], F[s & 3][k], acc[k]);
            SB();
            if (s < 15) F[(s + 3) & 3][k] = *(lds_u32x4*)(ob[0] + k * 18432 + (s + 3) * 1024);
            SB();
          }
      }
#pragma unroll 1
      for (int sp = 0; sp < SPIN; ++sp) {
        if (allok((bt[0].w ^ tag) | (bt[1].w ^ tag) | (w < 3 ? bt[2].w ^ tag : 0u) | (G[0].y ^ tag) | (G[1].y ^ tag) | (G[2].y ^ tag), true)) break;
        __builtin_amdgcn_s_sleep(1);
        OPQ(sb_); OPQ(s8_); OPQ(sg_[0]); OPQ(sg_[1]); OPQ(sg_[2]);
        ldbt();
      }
      {
        lds_u32* t0 = (lds_u32*)tw;
#pragma unroll
        for (int j = 0; j < 2; ++j) { t0[192 * j] = bt[j].x; t0[192 * j + 1] = bt[j].y; t0[192 * j + 2] = bt[j].z; }
        if (w < 3) { lds_u32* t1 = (lds_u32*)tw8; t1[0] = bt[2].x; t1[1] = bt[2].y; t1[2] = bt[2].z; }
      }
      u32x2 bv[4];
#pragma unroll
      for (int k = 0; k < 4; ++k) bv[k] = trread(tr[k]);
      auto mix = [&](f32x4 a, u32x2 b, u32 gq) {
        const float gg = lo16(gq), g1 = hi16(gq);
        return (u32x2){pk2(fmaf(gg, a[0], g1 * lo16(b.x)), fmaf(gg, a[1], g1 * hi16(b.x))), pk2(fmaf(gg, a[2], g1 * lo16(b.y)), fmaf(gg, a[3], g1 * hi16(b.y)))};
      };
      const rsrc_t ro = mkrsrc(out, S * D * 2);
      STO(swap16(mix(acc[1], bv[1], G[1].x), mix(acc[2], bv[2], G[2].x)), ro, vo0, so);
      STO(swap16(mix(acc[0], bv[0], G[0].x), mix(acc[3], bv[3], G[0].x)), ro, vo1, so);
    }
    return;
  }
  const u32 tag = mktag(ep);
  if (slot < 31) {
    const int ci = slot - 22;
    const int chunk = ci < 8 ? 8 * xcd + ci : 64 + xcd;
    const int d = chunk * 8 + w;
    const u32 vAre = *(const u32*)(Are + d * 128 + 2 * lane), vAim = *(const u32*)(Aim + d * 128 + 2 * lane);
    const u32 vBre = *(const u32*)(Bre + d * 128 + 2 * lane), vBim = *(const u32*)(Bim + d * 128 + 2 * lane);
    const u32 vCre = *(const u32*)(Cre + d * 128 + 2 * lane), vCim = *(const u32*)(Cim + d * 128 + 2 * lane);
    const u16 vld = logdt[d];
    const u16 vds = dskip[d];
    lds_vu32* cflag = (lds_vu32*)(sm + MOFF + 8192);
    {
      if (lane < 30) *(lds_u32x4*)(sm + UOFF + w * UW + lane * 16) = (u32x4){0u, 0u, 0u, 0u};
      if (tid == 0) *cflag = 0u;
      const int y = w >> 1, mm = w & 1;
      const int K = ((mm ? 0x0c0c1312 : 0x0c14150d) >> (8 * y)) & 255, len = ((mm ? 0x2275 : 0x2933) >> (4 * y)) & 15;
      const u32 V = w == 5 ? 0xffffu : 0x3f80u;
      const u32 bits = (u32)(((unsigned long long)((1u << len) - 1u) << (c + 8 * g + 24 - K)) >> 24);
      u32x4 r;
#pragma unroll
      for (int i = 0; i < 4; ++i) r[i] = __umul24(__umul24((bits >> (2 * i)) & 3u, 0x8001u) & 0x10001u, V);
      *(lds_u32x4*)(sm + MOFF + w * 1024 + lane * 16) = r;
      WAITLG0();
      BARRIER();
    }
    if (w < 4) __builtin_amdgcn_s_setprio(1);
    if (w == 7) __builtin_amdgcn_raw_buffer_store_b32(tag, rk, lane == 0 ? blockIdx.x * 4 : OOB, OFF_A, 16);
    u32x4 xv[2][4];
    u32 pf0, pf1;
    auto pfw = [&]() {
      const rsrc_t rw = mkrsrc(wo, D * D * 2);
      pf0 = __builtin_amdgcn_raw_buffer_load_b32(rw, lane * 128, (ci * 8 + w) * 9216, 0);
      pf1 = __builtin_amdgcn_raw_buffer_load_b32(rw, lane < 8 ? lane * 128 : OOB, (ci * 8 + w) * 9216 + 8192, 0);
    };
    lds_char* tV = sm + w * TBW;
    lds_char* si = tV + 16 * TROW;
    {
      const float dt = lo16(pk2(__builtin_amdgcn_exp2f(lo16(vld) * 1.44269504088896f), 0.f));
      f32x2 w1[2], w16[2], w256[2], cb[2];
#pragma unroll
      for (int m = 0; m < 2; ++m) {
        float ar = dt * (m ? hi16(vAre) : lo16(vAre)), ai = dt * (m ? hi16(vAim) : lo16(vAim));
        float br = dt * (m ? hi16(vBre) : lo16(vBre)), bi = dt * (m ? hi16(vBim) : lo16(vBim));
        float cr = m ? hi16(vCre) : lo16(vCre), cim = m ? hi16(vCim) : lo16(vCim);
        cb[m] = (f32x2){cr * br - cim * bi, -(cr * bi + cim * br)};
        float le = ar * 1.44269504088896f, rv = ai * 0.15915494309189535f;
        float e1 = __builtin_amdgcn_exp2f(le), e16 = __builtin_amdgcn_exp2f(le * 16.f), e256 = __builtin_amdgcn_exp2f(le * 256.f);
        float r1 = __builtin_amdgcn_fractf(rv), r16 = __builtin_amdgcn_fractf(rv * 16.f), r256 = __builtin_amdgcn_fractf(rv * 256.f);
        w1[m] = (f32x2){e1 * __builtin_amdgcn_cosf(r1), -e1 * __builtin_amdgcn_sinf(r1)};
        w16[m] = (f32x2){e16 * __builtin_amdgcn_cosf(r16), e16 * __builtin_amdgcn_sinf(r16)};
        w256[m] = (f32x2){e256 * __builtin_amdgcn_cosf(r256), -e256 * __builtin_amdgcn_sinf(r256)};
      }
      {
        int xo = w < 4 ? 4 * lane * 1152 : OOB;
        asm volatile("" : "+v"(xo) : "v"(w1[0]), "v"(w1[1]));
        if (w < 4) {
          const rsrc_t rx = mkrsrc(x, S * D * 2);
#pragma unroll
          for (int i = 0; i < 2; ++i)
#pragma unroll
            for (int j = 0; j < 4; ++j) xv[i][j] = __builtin_amdgcn_raw_buffer_load_b128(rx, xo + j * 1152, (w * 512 + i * 256) * 1152 + chunk * 16, 0);
        }
        SB();
      }
      auto put = [&](lds_char* t, int row, f32x2 a, f32x2 b) {
        *(lds_u32x2*)(t + row * TROW + lane * 8) = (u32x2){pk2(a.x, a.y), pk2(b.x, b.y)};
      };
      f32x2 ko[4][2];
      {
        f32x2 v0 = w16[0], v1 = w16[1];
        f32x2 w2[2] = {cmul(w1[0], w1[0]), cmul(w1[1], w1[1])};
        f32x2 u0 = cmul(cb[0], w256[0]), u1 = cmul(cb[1], w256[1]);
        *(lds_u32x2*)(tV + lane * 8) = (u32x2){0x00003f80u, 0x00003f80u};
        put(tV, 1, v0, v1);
        put(si, 8, u0, u1);
#pragma unroll
        for (int e = 2; e < 16; ++e) {
          v0 = cmul(v0, w16[0]); v1 = cmul(v1, w16[1]); put(tV, e, v0, v1);
          if (e < 10) {
            u0 = cmul(u0, w2[0]); u1 = cmul(u1, w2[1]);
            put(si, e - 2, u0, u1);
            if (e & 1) { ko[(e - 2) >> 1][0] = u0; ko[(e - 2) >> 1][1] = u1; }
          }
        }
      }
      const int fr = c * TROW + g * 16;
      u32x4 Vf[8];
      f32x4 t0 = z4, ta = z4, tbb = z4;
#pragma unroll
      for (int kk = 0; kk < 8; ++kk) {
        Vf[kk] = *(lds_u32x4*)(tV + fr + kk * 64);
        u32x4 af = *(lds_u32x4*)(si + fr + kk * 64);
        ta = mfma(af, Vf[kk], ta);
      }
      {
        f32x2 u0 = cb[0], u1 = cb[1];
        put(tV, 0, u0, u1);
#pragma unroll
        for (int e = 1; e < 16; ++e) { u0 = cmul(u0, w1[0]); u1 = cmul(u1, w1[1]); put(tV, e, u0, u1); }
      }
#pragma unroll
      for (int kk = 0; kk < 8; ++kk) t0 = mfma(*(lds_u32x4*)(tV + fr + kk * 64), Vf[kk], t0);
      const u32x4 zz = {0u, 0u, 0u, 0u};
      *(lds_u32x4*)(si + lane * 16) = zz;
      *(lds_u32x4*)(si + 1024 + lane * 16) = zz;
      if (lane < 4) *(lds_u32x4*)(si + 2048 + lane * 16) = zz;
#pragma unroll
      for (int o = 0; o < 4; ++o) {
        f32x2 a0 = cmul(ko[o][0], w256[0]), a1 = cmul(ko[o][1], w256[1]);
        put(tV, o, a0, a1);
        a0 = cmul(a0, w256[0]); a1 = cmul(a1, w256[1]);
        put(tV, 4 + o, a0, a1);
        if (o & 1) {
#pragma unroll
          for (int qq = 0; qq < 4; ++qq) {
            a0 = cmul(a0, w256[0]); a1 = cmul(a1, w256[1]);
            put(tV, 8 + 2 * qq + (o >> 1), a0, a1);
          }
        }
      }
#pragma unroll
      for (int kk = 0; kk < 8; ++kk) tbb = mfma(Vf[kk], *(lds_u32x4*)(tV + fr + kk * 64), tbb);
      *(lds_u32x2*)(si + 2 * (540 - 16 * c - 4 * g)) = (u32x2){pk2(t0[3], t0[2]), pk2(t0[1], t0[0])};
      if (g < 2) {
        lds_u32* pR = (lds_u32*)(si + 2 * (278 - 16 * c - 8 * g));
        pR[0] = pk2(ta[3], ta[3]); pR[1] = pk2(ta[3], ta[2]); pR[2] = pk2(ta[1], ta[1]); pR[3] = pk2(ta[1], ta[0]);
        if (lane == 31) *(lds_u16*)(si + 2 * 29) = (u16)pk2(ta[3], ta[3]);
      } else if (lane == 32) {
        *(lds_u32*)(si + 2 * 286) = pk2(ta[0], ta[0]);
      }
      {
        int p0 = c < 8 ? 560 + 64 * (c & 3) + 44 - 16 * (c >> 2) - 4 * g : 816 + 80 * (c & 1) + 60 - 16 * ((c - 8) >> 1) - 4 * g;
        *(lds_u32x2*)(si + 2 * p0) = (u32x2){pk2(tbb[3], tbb[2]), pk2(tbb[1], tbb[0])};
      }
#pragma unroll
      for (int i = 0; i < 2; ++i) {
        u32x4 a = *(lds_u32x4*)(si + i * 1024 + lane * 16);
        u32 n = *(lds_u32*)(si + i * 1024 + lane * 16 + 16);
        *(lds_u32x4*)(si + 2112 + i * 1024 + lane * 16) = (u32x4){__builtin_amdgcn_alignbit(a.y, a.x, 16), __builtin_amdgcn_alignbit(a.z, a.y, 16), __builtin_amdgcn_alignbit(a.w, a.z, 16), __builtin_amdgcn_alignbit(n, a.w, 16)};
      }
    }
    pfw();
    if (w < 4) {
      lds_char* ub = sm + UOFF + 480 + w * 1024 + lane * 8;
#pragma unroll
      for (int i = 0; i < 2; ++i)
#pragma unroll
        for (int k = 0; k < 4; ++k) {
          *(lds_u32x2*)(ub + i * 512 + (2 * k) * UW) = (u32x2){__builtin_amdgcn_perm(xv[i][1][k], xv[i][0][k], 0x05040100u), __builtin_amdgcn_perm(xv[i][3][k], xv[i][2][k], 0x05040100u)};
          *(lds_u32x2*)(ub + i * 512 + (2 * k + 1) * UW) = (u32x2){__builtin_amdgcn_perm(xv[i][1][k], xv[i][0][k], 0x07060302u), __builtin_amdgcn_perm(xv[i][3][k], xv[i][2][k], 0x07060302u)};
        }
      WAITLG0();
      ldsadd(cflag, lane);
    }
    asm volatile("" :: "s"(tag));
    lds_char* su = sm + UOFF + w * UW;
    lds_char* ub = su + 32 + 32 * c + 16 - 16 * g;
    lds_char* kb = si + ((c & 1) ? 2 * (551 - 8 * g - c) : 2112 + 2 * (550 - 8 * g - c)) - 960;
    lds_char* k16 = kb - 64;
    lds_char* sb = si + ((c & 1) ? 2 * (79 - c + 4 * g) : 2112 + 2 * (78 - c + 4 * g)) + 2 * 496;
    lds_char* mb = sm + MOFF + lane * 16;
    asm volatile("" : "+v"(ub), "+v"(kb), "+v"(k16), "+v"(sb));
    auto ufrag = [&](int s) { return *(lds_u32x4*)(ub + 448 + 64 * s); };
    auto kfrag = [&](int i) { lds_u32* pp = (lds_u32*)(i == 16 ? k16 : kb + 960 - 64 * i); return (u32x4){pp[0], pp[1], pp[2], pp[3]}; };
    auto sfrag = [&](int e0, int e1) { lds_u32* pp = (lds_u32*)(sb + 2 * (e0 - 496)); lds_u32* q2 = (lds_u32*)(sb + 2 * (e1 - 496)); return (u32x4){pp[0], pp[1], q2[0], q2[1]}; };
    ldswait(cflag, 4u);
    f32x4 acc[8];
    u32x4 UU[2][8], AA[2][2];
    auto pre = [&](int r, int T) { UU[r & 1][T] = ufrag(8 * T - r); };
    {
      u32x4 U[8], F2[6][2], F3[4], Sf[10];
#pragma unroll
      for (int m = 0; m < 8; ++m) U[m] = ufrag(8 * m);
      u32x4 A0 = kfrag(0), A1 = kfrag(8), A2 = kfrag(16);
      const u32x4 bd0 = *(lds_u32x4*)(mb), bd1 = *(lds_u32x4*)(mb + 1024), bd2 = *(lds_u32x4*)(mb + 2048), bd4 = *(lds_u32x4*)(mb + 3072), bd5 = *(lds_u32x4*)(mb + 4096), fixm = *(lds_u32x4*)(mb + 5120);
      u32 k1024 = *(lds_u16*)(si + 2 * (560 + 192 + 16));
      k1024 |= k1024 << 16;
#pragma unroll
      for (int T = 0; T < 8; ++T) acc[T] = mfma(A0, U[T], z4);
#pragma unroll
      for (int m = 0; m < 6; ++m) {
        u32x4 Us = *(lds_u32x4*)(ub + 448 + 64 * 8 * m - 16);
        f32x4 f0 = mfma(U[m], bd0, z4), f1 = mfma(U[m], bd1, z4), f2 = mfma(U[m], bd2, z4), f3 = mfma(Us, bd1, z4);
        F2[m][0] = (u32x4){pk2(f0[0], f0[1]), pk2(f0[2], f0[3]), pk2(f1[0], f1[1]), pk2(f1[2], f1[3])};
        F2[m][1] = (u32x4){pk2(f2[0], f2[1]), pk2(f2[2], f2[3]), pk2(f3[0], f3[1]), pk2(f3[2], f3[3])};
        if (m < 4) {
          f32x4 f4 = mfma(U[m], bd4, z4), f5 = mfma(Us, bd5, z4);
          F3[m] = (u32x4){pk2(f4[0], f4[1]), pk2(f4[2], f4[3]), pk2(f5[0], f5[1]), pk2(f5[2], f5[3])};
        }
      }
      SB();
#pragma unroll
      for (int T = 1; T < 8; ++T) {
        acc[T] = mfma(A1, U[T - 1], acc[T]);
        if (T < 7) { const int dl = 2 + (T - 1) / 2, pr = (T - 1) & 1; Sf[T - 1] = sfrag(560 + 128 * pr - 16 * dl, 560 + 128 * pr + 64 - 16 * dl); }
        SB();
      }
#pragma unroll
      for (int T = 2; T < 8; ++T) {
        acc[T] = mfma(A2, U[T - 2], acc[T]);
        if (T < 6) { const int dl = T + 2; Sf[4 + T] = sfrag(816 + 48 - 16 * dl, 816 + 80 + 48 - 16 * dl); }
        SB();
      }
      u32x4 fx = {fixm[0] & k1024, fixm[1] & k1024, fixm[2] & k1024, fixm[3] & k1024};
#pragma unroll
      for (int T = 4; T < 8; ++T) acc[T] = mfma(fx, U[T - 4], acc[T]);
      SB();
#pragma unroll
      for (int dl = 2; dl <= 4; ++dl)
#pragma unroll
        for (int pr = 0; pr < 2; ++pr)
#pragma unroll
          for (int T = dl; T < 8; ++T) {
            acc[T] = mfma(F2[T - dl][pr], Sf[2 * (dl - 2) + pr], acc[T]);
            if (dl == 4) pre(1, 4 * pr + T - 4);
            if (dl == 3 && T == 7) AA[1][pr] = kfrag(8 * pr + 1);
            SB();
          }
#pragma unroll
      for (int dl = 4; dl < 8; ++dl)
#pragma unroll
        for (int T = dl; T < 8; ++T) { acc[T] = mfma(F3[T - dl], Sf[2 + dl], acc[T]); SB(); }
    }
#pragma unroll
    for (int r = 1; r < 8; ++r) {
#pragma unroll
      for (int T = 0; T < 8; ++T) {
        acc[T] = mfma(AA[r & 1][0], UU[r & 1][T], acc[T]);
        if (r < 7) pre(r + 1, T);
        SB();
      }
#pragma unroll
      for (int T = 1; T < 8; ++T) {
        acc[T] = mfma(AA[r & 1][1], UU[r & 1][T - 1], acc[T]);
        if (r < 7 && T < 3) AA[(r + 1) & 1][T - 1] = kfrag(8 * (T - 1) + r + 1);
        SB();
      }
    }
    const float dsk = lo16(vds);
#pragma unroll
    for (int T = 0; T < 8; ++T) {
      lds_u32x2* pu = (lds_u32x2*)(su + 480 + 512 * T + 32 * c + 8 * g);
      u32x2 uu = *pu;
      *pu = (u32x2){pk2(fmaf(lo16(uu.x), dsk, acc[T][0]), fmaf(hi16(uu.x), dsk, acc[T][1])), pk2(fmaf(lo16(uu.y), dsk, acc[T][2]), fmaf(hi16(uu.y), dsk, acc[T][3]))};
    }
    {
      lds_u32* tp = (lds_u32*)(su + 480 + 12 * lane);
      u32x4 o[6];
#pragma unroll
      for (int i = 0; i < 6; ++i) o[i] = (u32x4){tp[192 * i], tp[192 * i + 1], tp[192 * i + 2], tag};
#pragma unroll
      for (int i = 0; i < 6; ++i) asm volatile("" : "+v"(o[i]));
#pragma unroll
      for (int i = 0; i < 6; ++i) st16(o[i], rk, i == 5 && lane >= 24 ? OOB : lane * 16 + (i & 3) * 1024, OFF_BT + d * 5504 + (i >> 2) * 4096);
    }
    asm volatile("" :: "v"(pf0), "v"(pf1));
    return;
  }
  {
    unsigned long long pc0;
    u32 pof;
    asm volatile("s_getpc_b64 %0\n.Lpc%=:\n s_mov_b32 %1, .Lpc%=-k1" : "=s"(pc0), "=s"(pof));
    const u32 cpf = __builtin_amdgcn_raw_buffer_load_b32(mkrsrc((const void*)(pc0 - pof), CODEPF), tid * 128, 0, 0);
    const int row0 = 256 * xcd + 32 * w;
    const char* xs = (const char*)x + (size_t)(row0 + (lane >> 3)) * 1152 + (lane & 7) * 16;
    u32x4 gf[9], xf[4][9];
#pragma unroll
    for (int k = 0; k < 9; ++k) gf[k] = *(const u32x4*)((const char*)gw + k * 128 + (lane & 7) * 16);
#pragma unroll
    for (int i = 0; i < 4; ++i)
#pragma unroll
      for (int k = 0; k < 9; ++k) xf[i][k] = *(const u32x4*)(xs + i * 8 * 1152 + k * 128);
    SB();
    float sv = 0.f;
#pragma unroll
    for (int i = 0; i < 4; ++i) {
      float a[4] = {0.f, 0.f, 0.f, 0.f};
#pragma unroll
      for (int k = 0; k < 9; ++k)
#pragma unroll
        for (int e = 0; e < 4; ++e)
        {
          const u32 xa = xf[i][k][e], ga = gf[k][e];
          a[e] = __builtin_amdgcn_fdot2_f32_bf16(__builtin_bit_cast(bf16x2, xa), __builtin_bit_cast(bf16x2, ga), a[e], false);
        }
      float t = (a[0] + a[1]) + (a[2] + a[3]);
      t += __uint_as_float(__builtin_amdgcn_update_dpp(0u, __float_as_uint(t), 0xB1, 0xf, 0xf, true));
      t += __uint_as_float(__builtin_amdgcn_update_dpp(0u, __float_as_uint(t), 0x4E, 0xf, 0xf, true));
      t += __uint_as_float(__builtin_amdgcn_update_dpp(0u, __float_as_uint(t), 0x141, 0xf, 0xf, true));
      sv = (lane & 3) == i ? t : sv;
    }
    const float gs = lo16(pk2(sv, 0.f));
    const float gg = lo16(pk2(__builtin_amdgcn_rcpf(1.f + __builtin_amdgcn_exp2f(-gs * 1.44269504088896f)), 0.f));
    __builtin_amdgcn_raw_buffer_store_b64((u32x2){pk2(gg, 1.f - gg), tag}, rk, (lane & 4) == 0 ? (row0 + 8 * (lane & 3) + (lane >> 3)) * 8 : OOB, OFF_G, 16);
    asm volatile("" :: "v"(cpf));
    if (blockIdx.x == 255 && w == 7) {
      const int vq = lane < 48 ? (lane / 3) * 8 * 24576 + (lane % 3) * 8192 + 12 : OOB;
      const int vb = lane < 8 ? lane * 44032 + 12 : OOB;
#pragma unroll 1
      for (int sp = 0; sp < SPIN; ++sp) {
        int z0 = 0;
        OPQ(z0);
        const u32x4 t0 = __builtin_amdgcn_raw_buffer_load_b128(rk, lane * 16, z0 + OFF_A, 16);
        const u32 t5 = __builtin_amdgcn_raw_buffer_load_b32(rk, lane * 256 + 4, z0 + OFF_G, 16);
        const u32 bad = (lane < 62 ? (t0.x ^ tag) | (t0.y ^ tag) | (t0.z ^ tag) | (t0.w ^ tag) : 0u) | (t5 ^ tag);
        if (allok(bad, true)) break;
        __builtin_amdgcn_s_sleep(16);
      }
      __builtin_amdgcn_raw_buffer_store_b32(ep + 1u, rk, lane == 0 ? 0 : OOB, OFF_E, 16);
    }
  }
}

extern "C" void kernel_launch(void* const* d_in, const int* in_sizes, int n_in, void* d_out, int out_size, void* d_ws,
                              size_t ws_size, hipStream_t stream) {
  (void)hipFuncSetAttribute((const void*)k1, hipFuncAttributeMaxDynamicSharedMemorySize, K1_LDS);
  k1<<<256, 512, K1_LDS, stream>>>((const u16*)d_in[0], (const u16*)d_in[1], (const u16*)d_in[2], (const u16*)d_in[4], (const u16*)d_in[5],
                                   (const u16*)d_in[6], (const u16*)d_in[7], (const u16*)d_in[8], (const u16*)d_in[9],
                                   (const u16*)d_in[10], (const u16*)d_in[11], (const u16*)d_in[12], (char*)d_ws, (u16*)d_out);
}
```
